```python
import torch

def _softmax(x, axis=-1):
    return torch.softmax(x, dim=axis)
def _gelu(x):
    return torch.nn.functional.gelu(x, approximate="tanh")
def _silu(x):
    return torch.nn.functional.silu(x)
def _sigmoid(x):
    return torch.sigmoid(x)
def _layernorm(x, axis=-1, eps=1e-5):
    m = x.mean(dim=axis, keepdim=True)
    v = x.var(dim=axis, keepdim=True, unbiased=False)
    return (x - m) / torch.sqrt(v + eps)
def _scatter_add(tgt, idx, src, axis=0):
    return tgt.clone().index_add_(axis, idx, src)
def _segment_sum(src, seg, n):
    out = torch.zeros((n,) + src.shape[1:], dtype=src.dtype)
    return out.index_add_(0, seg, src)
def _pad(x, pw):
    flat = tuple(v for pair in reversed(pw) for v in pair)
    return torch.nn.functional.pad(x, flat)

kernel_name = 'gated_mix__gqa_block_cached__compressed_kv_cla__decode'

def setup_inputs():

    _w = (lambda a: (lambda t: t.to(torch.bfloat16) if t.is_floating_point() else t)(torch.as_tensor(a)))
    def _A_setup():
        B, Sq, D = 2, 1, 576
        Hq, Hkv, Sk, Dh = 32, 16, 4096, 128
        assert Hq % Hkv == 0, (Hq, Hkv)
        return {
            "x": _w((lambda *s: torch.randn(*s, dtype=torch.bfloat16) * 0.05)(B, Sq, D)),
            "k_cache": _w((lambda *s: torch.randn(*s, dtype=torch.bfloat16) * 6.4)(B, Hkv, Sk, Dh)),
            "v_cache": _w((lambda *s: torch.randn(*s, dtype=torch.bfloat16) * 0.8)(B, Hkv, Sk, Dh)),
            "wq": _w((lambda *s: torch.randn(*s, dtype=torch.bfloat16) * 6.4)(D, Hq * Dh) * (D ** -0.5)),
            "wo": _w((lambda *s: torch.randn(*s, dtype=torch.bfloat16) * 0.8)(Hq * Dh, D) * ((Hq * Dh) ** -0.5)),
        }
    _A_in = _A_setup()

    def _B_setup():
        B, H, Sq, Sk, D, M = 2, 32, 1, 4096, 128, 576
        Dc = 4 * D
        return {
            "x": _w((lambda *s: torch.randn(*s, dtype=torch.bfloat16) * 0.05)(B, Sq, M)),
            "c_kv": _w((lambda *s: torch.randn(*s, dtype=torch.bfloat16) * 0.8)(B, Sk, Dc)),
            "w_q": _w((lambda *s: torch.randn(*s, dtype=torch.bfloat16) * 6.4)(M, H, D) * (M ** -0.5)),
            "w_uk": _w((lambda *s: torch.randn(*s, dtype=torch.bfloat16) * 6.4)(Dc, H, D) * (Dc ** -0.5)),
            "w_uv": _w((lambda *s: torch.randn(*s, dtype=torch.bfloat16) * 0.8)(Dc, H, D) * (Dc ** -0.5)),
            "w_o": _w((lambda *s: torch.randn(*s, dtype=torch.bfloat16) * 0.8)(H, D, M) * ((H * D) ** -0.5)),
        }
    _B_in = _B_setup()

    return {
        "A_x": _A_in["x"],
        "A_k_cache": _A_in["k_cache"],
        "A_v_cache": _A_in["v_cache"],
        "A_wq": _A_in["wq"],
        "A_wo": _A_in["wo"],
        "B_x": _B_in["x"],
        "B_c_kv": _B_in["c_kv"],
        "B_w_q": _B_in["w_q"],
        "B_w_uk": _B_in["w_uk"],
        "B_w_uv": _B_in["w_uv"],
        "B_w_o": _B_in["w_o"],
        "gate_w": _w((lambda *s: torch.randn(*s, dtype=torch.bfloat16) * 0.05)(576) * 16.0),
    }

def reference(A_x, A_k_cache, A_v_cache, A_wq, A_wo, B_x, B_c_kv, B_w_q, B_w_uk, B_w_uv, B_w_o, gate_w):
    def _A_ref(x, k_cache, v_cache, wq, wo):
        B, Sq, D = x.shape
        Hq, Dh = 32, 128
        Hkv = k_cache.shape[1]
        rep = Hq // Hkv
        q = (lambda x, *d: x.permute(*d))((x @ wq).reshape(B, Sq, Hq, Dh), 0, 2, 1, 3)
        k = (lambda x, n, axis: torch.repeat_interleave(x, n, dim=axis))(k_cache, rep, axis=1)
        v = (lambda x, n, axis: torch.repeat_interleave(x, n, dim=axis))(v_cache, rep, axis=1)
        scores = torch.einsum("bhqd,bhkd->bhqk", q, k) * (Dh ** -0.5)
        p = _softmax(scores, axis=-1)
        o = torch.einsum("bhqk,bhkd->bhqd", p, v)
        return (lambda x, *d: x.permute(*d))(o, 0, 2, 1, 3).reshape(B, Sq, Hq * Dh) @ wo

    def _B_ref(x, c_kv, w_q, w_uk, w_uv, w_o):
        B, Sq, M = x.shape[0], x.shape[1], x.shape[2]
        Sk = c_kv.shape[1]
        H, D = w_q.shape[1], w_q.shape[2]
        q = torch.einsum("bqm,mhd->bhqd", x, w_q)
        k = torch.einsum("bkc,chd->bhkd", c_kv, w_uk)
        v = torch.einsum("bkc,chd->bhkd", c_kv, w_uv)
        scores = torch.einsum("bhqd,bhkd->bhqk", q, k) * (D ** -0.5)
        off = Sk - Sq
        qi = torch.arange(Sq)[:, None]
        kj = torch.arange(Sk)[None, :]
        cmask = kj <= (qi + off)
        scores = torch.where(cmask[None, None], scores, -1e9)
        p = _softmax(scores, axis=-1)
        ctx = torch.einsum("bhqk,bhkd->bhqd", p, v)
        return torch.einsum("bhqd,hdm->bqm", ctx, w_o)

    x = A_x
    g = _sigmoid((x * gate_w).sum(-1, keepdims=True))
    a = _A_ref(x, A_k_cache, A_v_cache, A_wq, A_wo)
    b = _B_ref(x, B_c_kv, B_w_q, B_w_uk, B_w_uv, B_w_o)
    return g * a + (1 - g) * b
```

```cpp
#include <hip/hip_runtime.h>
#include <hip/hip_bf16.h>
#include <stdint.h>

typedef unsigned short u16;
__device__ __forceinline__ float bf2f(u16 v) { return __uint_as_float(((unsigned)v) << 16); }
__device__ __forceinline__ float rbf(float f) { return __bfloat162float(__float2bfloat16(f)); }

__global__ void k_qproj(const u16* x, const u16* w, float* q) {
  int n = blockIdx.x * blockDim.x + threadIdx.x;
  float a0 = 0.f, a1 = 0.f;
  for (int m = 0; m < 576; ++m) { float wv = bf2f(w[(size_t)m * 4096 + n]); a0 += bf2f(x[m]) * wv; a1 += bf2f(x[576 + m]) * wv; }
  q[n] = rbf(a0); q[4096 + n] = rbf(a1);
}
__global__ void k_qabs(const float* q, const u16* wuk, float* qabs) {
  int c = blockIdx.x, h = blockIdx.y, d = threadIdx.x;
  __shared__ float s0[128], s1[128];
  float wv = bf2f(wuk[((size_t)c * 32 + h) * 128 + d]);
  s0[d] = wv * q[h * 128 + d]; s1[d] = wv * q[4096 + h * 128 + d];
  __syncthreads();
  if (d == 0) { float a = 0, b = 0; for (int i = 0; i < 128; ++i) { a += s0[i]; b += s1[i]; } qabs[(0 * 32 + h) * 512 + c] = a; qabs[(1 * 32 + h) * 512 + c] = b; }
}
__global__ void k_scoreA(const float* q, const u16* K, float* s) {
  int k = blockIdx.x * blockDim.x + threadIdx.x, h = blockIdx.y, b = blockIdx.z;
  const u16* kr = K + (((size_t)b * 16 + h / 2) * 4096 + k) * 128;
  float a = 0; for (int d = 0; d < 128; ++d) a += q[b * 4096 + h * 128 + d] * bf2f(kr[d]);
  s[((size_t)b * 32 + h) * 4096 + k] = a * 0.08838834764831845f;
}
__global__ void k_scoreB(const float* qabs, const u16* C, float* s) {
  int k = blockIdx.x * blockDim.x + threadIdx.x, h = blockIdx.y, b = blockIdx.z;
  const u16* kr = C + ((size_t)b * 4096 + k) * 512;
  float a = 0; for (int d = 0; d < 512; ++d) a += qabs[(b * 32 + h) * 512 + d] * bf2f(kr[d]);
  s[((size_t)b * 32 + h) * 4096 + k] = a * 0.08838834764831845f;
}
__global__ void k_softmax(float* s) {
  float* r = s + (size_t)blockIdx.x * 4096;
  __shared__ float red[256];
  int t = threadIdx.x; float m = -INFINITY;
  for (int i = t; i < 4096; i += 256) m = fmaxf(m, r[i]);
  red[t] = m; __syncthreads();
  for (int o = 128; o; o >>= 1) { if (t < o) red[t] = fmaxf(red[t], red[t + o]); __syncthreads(); }
  m = red[0]; __syncthreads();
  float l = 0; for (int i = t; i < 4096; i += 256) { float e = __expf(r[i] - m); r[i] = e; l += e; }
  red[t] = l; __syncthreads();
  for (int o = 128; o; o >>= 1) { if (t < o) red[t] += red[t + o]; __syncthreads(); }
  l = 1.f / red[0];
  for (int i = t; i < 4096; i += 256) r[i] *= l;
}
__global__ void k_pvA(const float* p, const u16* V, float* o) {
  int h = blockIdx.x, b = blockIdx.y, d = threadIdx.x;
  const u16* v = V + ((size_t)b * 16 + h / 2) * 4096 * 128;
  const float* pr = p + ((size_t)b * 32 + h) * 4096;
  float a = 0; for (int k = 0; k < 4096; ++k) a += pr[k] * bf2f(v[(size_t)k * 128 + d]);
  o[b * 4096 + h * 128 + d] = a;
}
__global__ void k_pvB(const float* p, const u16* C, float* o) {
  int h = blockIdx.x, b = blockIdx.y, d = threadIdx.x;
  const u16* v = C + (size_t)b * 4096 * 512;
  const float* pr = p + ((size_t)b * 32 + h) * 4096;
  float a = 0; for (int k = 0; k < 4096; ++k) a += pr[k] * bf2f(v[(size_t)k * 512 + d]);
  o[(b * 32 + h) * 512 + d] = a;
}
__global__ void k_uv(const float* olat, const u16* wuv, float* ctx) {
  int h = blockIdx.x, b = blockIdx.y, d = threadIdx.x;
  float a = 0; for (int c = 0; c < 512; ++c) a += olat[(b * 32 + h) * 512 + c] * bf2f(wuv[((size_t)c * 32 + h) * 128 + d]);
  ctx[b * 4096 + h * 128 + d] = a;
}
__global__ void k_out(const u16* x, const u16* gw, const float* oA, const float* ctx, const u16* woA, const u16* woB, __hip_bfloat16* out) {
  int n = blockIdx.x * blockDim.x + threadIdx.x, b = blockIdx.y;
  float g = 0; for (int m = 0; m < 576; ++m) g += bf2f(x[b * 576 + m]) * bf2f(gw[m]);
  g = 1.f / (1.f + __expf(-g));
  float a = 0, c = 0;
  for (int r = 0; r < 4096; ++r) { a += oA[b * 4096 + r] * bf2f(woA[(size_t)r * 576 + n]); c += ctx[b * 4096 + r] * bf2f(woB[(size_t)r * 576 + n]); }
  out[b * 576 + n] = __float2bfloat16(g * a + (1.f - g) * c);
}

extern "C" void kernel_launch(void* const* d_in, const int* in_sizes, int n_in,
                              void* d_out, int out_size, void* d_ws, size_t ws_size,
                              hipStream_t stream) {
  const u16* x = (const u16*)d_in[0]; const u16* K = (const u16*)d_in[1]; const u16* V = (const u16*)d_in[2];
  const u16* wqA = (const u16*)d_in[3]; const u16* woA = (const u16*)d_in[4];
  const u16* C = (const u16*)d_in[6]; const u16* wqB = (const u16*)d_in[7]; const u16* wuk = (const u16*)d_in[8];
  const u16* wuv = (const u16*)d_in[9]; const u16* woB = (const u16*)d_in[10]; const u16* gw = (const u16*)d_in[11];
  float* ws = (float*)d_ws;
  float* qA = ws; float* qB = qA + 8192; float* qabs = qB + 8192; float* sA = qabs + 32768; float* sB = sA + 262144;
  float* oA = sB + 262144; float* olat = oA + 8192; float* ctx = olat + 32768;
  k_qproj<<<64, 64, 0, stream>>>(x, wqA, qA);
  k_qproj<<<64, 64, 0, stream>>>(x, wqB, qB);
  k_qabs<<<dim3(512, 32), 128, 0, stream>>>(qB, wuk, qabs);
  k_scoreA<<<dim3(64, 32, 2), 64, 0, stream>>>(qA, K, sA);
  k_scoreB<<<dim3(64, 32, 2), 64, 0, stream>>>(qabs, C, sB);
  k_softmax<<<128, 256, 0, stream>>>(sA);
  k_pvA<<<dim3(32, 2), 128, 0, stream>>>(sA, V, oA);
  k_pvB<<<dim3(32, 2), 512, 0, stream>>>(sB, C, olat);
  k_uv<<<dim3(32, 2), 128, 0, stream>>>(olat, wuv, ctx);
  k_out<<<dim3(9, 2), 64, 0, stream>>>(x, gw, oA, ctx, woA, woB, (__hip_bfloat16*)d_out);
}
```

```cpp
#include <hip/hip_runtime.h>
#include <hip/hip_bf16.h>
#include <stdint.h>

namespace {

typedef unsigned short u16;
typedef unsigned int u32;
typedef unsigned long long u64;
typedef u32 u32x4 __attribute__((ext_vector_type(4)));
typedef u32 u32x2 __attribute__((ext_vector_type(2)));
typedef float f32x4 __attribute__((ext_vector_type(4)));
typedef float f32x2 __attribute__((ext_vector_type(2)));
typedef float f32x16 __attribute__((ext_vector_type(16)));
typedef short s16x4 __attribute__((ext_vector_type(4)));
typedef __bf16 bf16x2 __attribute__((ext_vector_type(2)));
typedef __bf16 bf16x8 __attribute__((ext_vector_type(8)));
#define GAS __attribute__((address_space(1)))
#define LAS __attribute__((address_space(3)))
#define RLX_AGENT __ATOMIC_RELAXED, __HIP_MEMORY_SCOPE_AGENT
#define DI __device__ __forceinline__

constexpr u32 WS_EPOCH = 0;
constexpr u32 WS_F1FLAG = 256;
constexpr u32 WS_F2FLAG = 1024;
constexpr u32 WS_F2FLAG2 = 2048;
constexpr u32 WS_AFLAG = 3072;
constexpr u32 WS_QA = 8192;
constexpr u32 WS_QABS = 32768;
constexpr u32 WS_ML = WS_QABS + 131072;
constexpr u32 WS_OLAT = WS_ML + 16384;
constexpr u32 WS_AG = WS_OLAT + 2097152;
constexpr u32 WS_OG = WS_AG + 64 * 8 * 66 * 8;
constexpr u32 WS_END = WS_OG + 2 * 64 * 64 * 18 * 8;

constexpr int LDS_BYTES = 163840;
constexpr u32 QOFF = 131072;
constexpr float C2 = 0.08838834764831845f * 1.4426950408889634f;
constexpr u32 SPIN_CAP = 1u << 18;
#define A_SLOW 3
#define A_SLOWD 1

#define BAR() asm volatile("s_waitcnt lgkmcnt(0)\n\ts_barrier" ::: "memory")
#define DRAIN() asm volatile("s_waitcnt vmcnt(0)" ::: "memory")
#define SCHED() __builtin_amdgcn_sched_barrier(0)

struct Params {
  const u16 *x, *K, *V, *wqA, *woA, *ckv, *wqB, *wuk, *wuv, *woB, *gw;
  u16* out;
  unsigned char* ws;
};

DI float bflo(u32 v) { return __uint_as_float(v << 16); }
DI float bfhi(u32 v) { return __uint_as_float(v & 0xffff0000u); }
DI u32 pk2(float lo, float hi) {
  bf16x2 r; r[0] = (__bf16)lo; r[1] = (__bf16)hi; return __builtin_bit_cast(u32, r);
}
DI float dot2(u32 a, u32 b, float acc) {
  return __builtin_amdgcn_fdot2_f32_bf16(__builtin_bit_cast(bf16x2, a), __builtin_bit_cast(bf16x2, b), acc, false);
}
DI float dot8(u32x4 a, u32x4 b) {
  float d = dot2(a[0], b[0], 0.f); d = dot2(a[1], b[1], d); d = dot2(a[2], b[2], d); return dot2(a[3], b[3], d);
}
template <int CTRL> DI float dpp(float x) {
  return __builtin_bit_cast(float, __builtin_amdgcn_mov_dpp(__builtin_bit_cast(int, x), CTRL, 0xf, 0xf, true));
}
constexpr int XOR1 = 0xB1, XOR2 = 0x4E, XOR7 = 0x141, ROR8 = 0x128, ROR4 = 0x124;
DI float x16_max(float x) {
  auto s = __builtin_amdgcn_permlane16_swap(__float_as_uint(x), __float_as_uint(x), false, false);
  return fmaxf(__uint_as_float(s[0]), __uint_as_float(s[1]));
}
DI float x32_max(float x) {
  auto s = __builtin_amdgcn_permlane32_swap(__float_as_uint(x), __float_as_uint(x), false, false);
  return fmaxf(__uint_as_float(s[0]), __uint_as_float(s[1]));
}
DI float x16_sum(float x) {
  auto s = __builtin_amdgcn_permlane16_swap(__float_as_uint(x), __float_as_uint(x), false, false);
  return __uint_as_float(s[0]) + __uint_as_float(s[1]);
}
DI float x32_sum(float x) {
  auto s = __builtin_amdgcn_permlane32_swap(__float_as_uint(x), __float_as_uint(x), false, false);
  return __uint_as_float(s[0]) + __uint_as_float(s[1]);
}
DI float ex2(float x) { return __builtin_amdgcn_exp2f(x); }

DI u32x4 ldg16(const u16* p) { return *(const GAS u32x4*)p; }
DI u32x4 ldg16nt(const u16* p) { return __builtin_nontemporal_load((const GAS u32x4*)p); }
DI u32x4 ld_sc1(__amdgpu_buffer_rsrc_t rs, u32 off) { return __builtin_amdgcn_raw_buffer_load_b128(rs, off, 0, 16); }
DI void st_sc1(__amdgpu_buffer_rsrc_t rs, u32 off, u32x4 v) { __builtin_amdgcn_raw_buffer_store_b128(v, rs, off, 0, 16); }
DI u64 ld8_sc1(unsigned char* ws, u32 off) { return __hip_atomic_load((GAS u64*)(ws + off), RLX_AGENT); }
DI void st8_sc1(unsigned char* ws, u32 off, u64 v) { __hip_atomic_store((GAS u64*)(ws + off), v, RLX_AGENT); }

DI u64 mix64(u64 e) {
  u64 z = e + 0x9E3779B97F4A7C15ull;
  z = (z ^ (z >> 30)) * 0xBF58476D1CE4E5B9ull;
  z = (z ^ (z >> 27)) * 0x94D049BB133111EBull;
  return z ^ (z >> 31);
}
DI u64 tag_of(u64 e) { return mix64(e); }
DI void poll_flags(unsigned char* ws, u32 off, bool active, u64 tag) {
  for (u32 spins = 0; spins < SPIN_CAP; ++spins) {
    bool ok = true;
    if (active) ok = (ld8_sc1(ws, off) == tag);
    if (__all(ok)) break;
    __builtin_amdgcn_s_sleep(2);
  }
  __builtin_amdgcn_fence(__ATOMIC_ACQUIRE, "wavefront");
}
DI f32x2 spin_pair(__amdgpu_buffer_rsrc_t rs, u32 off, u32 tag32) {
  u32x4 v;
  for (u32 spins = 0; spins < SPIN_CAP; ++spins) {
    asm volatile("" ::: "memory");
    v = ld_sc1(rs, off);
    bool ok = (v[1] == tag32) & (v[3] == tag32);
    if (__all(ok)) break;
  }
  f32x2 r; r[0] = __uint_as_float(v[0]); r[1] = __uint_as_float(v[2]); return r;
}
DI void glds16(const void* gsrc, u32 lds_dst) {
  u32 keep;
  asm volatile("s_mov_b32 %0, m0\n\ts_mov_b32 m0, %2\n\ts_nop 0\n\tglobal_load_lds_dwordx4 %1, off nt\n\ts_mov_b32 m0, %0"
               : "=&s"(keep) : "v"(gsrc), "s"(lds_dst) : "memory");
}
DI u32 off_b(u32 row, u32 ch) { return 256u * row + 16u * (ch ^ (((row & 3) << 2) | ((row >> 2) & 3))); }

#define STAMP(i) do {} while (0)

struct Gemv1 { u32x4 w[8]; u32x4 x0, x1; };
DI void gemv1_issue(Gemv1& g, const u16* W, const u16* x, int t) {
  const int rg = t >> 3, ch = t & 7;
  const u16* wp = W + (size_t)(8 * rg) * 4096 + 8 * ch;
#pragma unroll
  for (int k = 0; k < 8; ++k) g.w[k] = ldg16nt(wp + (size_t)k * 4096);
  g.x0 = ldg16(x + 8 * rg);
  g.x1 = ldg16(x + 576 + 8 * rg);
}
DI void gemv1_finish(Gemv1& g, float* red  , int wave, int lane) {
  float a0[8], a1[8];
#pragma unroll
  for (int e = 0; e < 8; ++e) { a0[e] = 0.f; a1[e] = 0.f; }
#pragma unroll
  for (int k = 0; k < 8; ++k) {
    const float xa = (k & 1) ? bfhi(g.x0[k >> 1]) : bflo(g.x0[k >> 1]);
    const float xb = (k & 1) ? bfhi(g.x1[k >> 1]) : bflo(g.x1[k >> 1]);
#pragma unroll
    for (int e = 0; e < 8; ++e) {
      const float wf = (e & 1) ? bfhi(g.w[k][e >> 1]) : bflo(g.w[k][e >> 1]);
      a0[e] = fmaf(xa, wf, a0[e]); a1[e] = fmaf(xb, wf, a1[e]);
    }
  }
#pragma unroll
  for (int e = 0; e < 8; ++e) {
    a0[e] += dpp<ROR8>(a0[e]); a0[e] = x32_sum(x16_sum(a0[e]));
    a1[e] += dpp<ROR8>(a1[e]); a1[e] = x32_sum(x16_sum(a1[e]));
  }
  if (lane < 8) {
    float* r = red + wave * 128 + lane * 8;
    *(f32x4*)(r) = f32x4{a0[0], a0[1], a0[2], a0[3]};
    *(f32x4*)(r + 4) = f32x4{a0[4], a0[5], a0[6], a0[7]};
    *(f32x4*)(r + 64) = f32x4{a1[0], a1[1], a1[2], a1[3]};
    *(f32x4*)(r + 68) = f32x4{a1[4], a1[5], a1[6], a1[7]};
  }
}
DI u32x4 gemv1_sum(const float* red, int t) {
  f32x4 s0 = {0.f, 0.f, 0.f, 0.f}, s1 = {0.f, 0.f, 0.f, 0.f};
  const float* r = red + (t >> 3) * 64 + (t & 7) * 8;
#pragma unroll
  for (int v = 0; v < 9; ++v) { s0 += *(const f32x4*)(r + v * 128); s1 += *(const f32x4*)(r + v * 128 + 4); }
  return u32x4{pk2(s0[0], s0[1]), pk2(s0[2], s0[3]), pk2(s1[0], s1[1]), pk2(s1[2], s1[3])};
}
DI u32 og_off(int fam, int p, int t) {
  const int o = 2 * t, r = o / 18, pos = o - r * 18;
  return WS_OG + (u32)((((fam * 64 + r) * 64 + p) * 18 + pos) * 8);
}
DI void oproj_issue(u32x4 (&w)[8], const u16* W  , int tp) {
#pragma unroll
  for (int k = 0; k < 8; ++k) w[k] = ldg16nt(W + (size_t)k * 4608 + (size_t)tp * 8);
}
DI void oproj_compute(const u32x4 (&w)[8], const float* zt  , float* red  , int tp) {
  const int rg = tp / 72, c = tp - rg * 72;
  float a0[8], a1[8];
#pragma unroll
  for (int e = 0; e < 8; ++e) { a0[e] = 0.f; a1[e] = 0.f; }
#pragma unroll
  for (int k2 = 0; k2 < 4; ++k2) {
    const f32x4 z = *(const f32x4*)(zt + rg * 16 + k2 * 4);
#pragma unroll
    for (int kk = 0; kk < 2; ++kk) {
      const int k = 2 * k2 + kk;
      const float za = z[2 * kk], zb = z[2 * kk + 1];
#pragma unroll
      for (int e = 0; e < 8; ++e) {
        const float wf = (e & 1) ? bfhi(w[k][e >> 1]) : bflo(w[k][e >> 1]);
        a0[e] = fmaf(za, wf, a0[e]); a1[e] = fmaf(zb, wf, a1[e]);
      }
    }
  }
  float* r = red + rg * 1152 + c * 8;
  *(f32x4*)(r) = f32x4{a0[0], a0[1], a0[2], a0[3]};
  *(f32x4*)(r + 4) = f32x4{a0[4], a0[5], a0[6], a0[7]};
  *(f32x4*)(r + 576) = f32x4{a1[0], a1[1], a1[2], a1[3]};
  *(f32x4*)(r + 580) = f32x4{a1[4], a1[5], a1[6], a1[7]};
}
DI void oproj_publish(const float* red, __amdgpu_buffer_rsrc_t rs, int fam, int p, u32 tag32, int t) {
  f32x2 s = {0.f, 0.f};
#pragma unroll
  for (int rg = 0; rg < 8; ++rg) s += *(const f32x2*)(red + rg * 1152 + 2 * t);
  st_sc1(rs, og_off(fam, p, t), u32x4{__float_as_uint(s[0]), tag32, __float_as_uint(s[1]), tag32});
}

DI void role_A(const Params& p, unsigned char* smem, __amdgpu_buffer_rsrc_t rs, u64 epoch, int a) {
  const int t = threadIdx.x, lane = t & 63, wave = __builtin_amdgcn_readfirstlane(t >> 6);
  const int r = lane >> 4, c = lane & 15;
  const int hkv = a & 15, b = (a >> 4) & 1, s = a >> 5;
  const size_t base = ((size_t)(b * 16 + hkv) * 4096 + s * 1024 + wave * 64 + r) * 128 + c * 8;
  const u16* Kp = p.K + base;
  const u16* Vp = p.V + base;
  unsigned char* qs = smem;
  float* pw = (float*)(smem + 1024) + wave * 128;
  float* mrg = (float*)(smem + 1024 + 8192);
  float* mls = mrg + 16 * 256;
  u32x4 kv[16];
  u64 tag;
  if (wave == 0) {
    tag = tag_of(epoch);
    const u32 ntag = ~(u32)tag;
#pragma unroll
    for (int k = 0; k < 3; ++k) {
      const int n = lane + 64 * k;
      if (n < 132) {
        const int seg = n / 33, pos = n - seg * 33;
        st_sc1(rs, WS_AG + (u32)((((hkv * 4 + seg) * 8 + b * 4 + s) * 66 + 2 * pos) * 8), u32x4{0u, ntag, 0u, ntag});
      }
    }
    DRAIN();
    if (lane == 0) st8_sc1(p.ws, WS_AFLAG + a * 8, tag);
    poll_flags(p.ws, WS_F2FLAG + (hkv * 4 + (lane & 3)) * 8, lane < 4, tag);
    if (lane < 32) {
      const u32x4 q = ld_sc1(rs, WS_QA + (u32)((b * 4096 + hkv * 256 + lane * 8) * 2));
      *(u32x4*)(qs + lane * 16) = q;
    }
    STAMP(1);
    BAR();
#pragma unroll
    for (int i = 0; i < 16; ++i) kv[i] = ldg16nt(Kp + i * 512);
  } else {
#pragma unroll
    for (int i = 0; i < 16; ++i) {
      kv[i] = ldg16nt(Kp + i * 512);
      if (i < A_SLOW) {
        if (A_SLOWD == 1) asm volatile("s_waitcnt vmcnt(0)" ::: "memory");
        else if (i >= 1) asm volatile("s_waitcnt vmcnt(1)" ::: "memory");
      } else if (i >= 3 && i < 15) asm volatile("s_waitcnt vmcnt(3)" ::: "memory");
    }
    tag = tag_of(epoch);
    STAMP(1);
    BAR();
  }
  STAMP(2);
  const u32x4 q0 = *(const u32x4*)(qs + c * 16), q1 = *(const u32x4*)(qs + 256 + c * 16);
  float sc[2];
#pragma unroll
  for (int G = 0; G < 2; ++G) {
    float x[16];
#pragma unroll
    for (int ii = 0; ii < 8; ++ii) {
      const int i = 8 * G + ii;
      x[2 * ii] = dot8(kv[i], q0);
      x[2 * ii + 1] = dot8(kv[i], q1);
      SCHED();
      kv[i] = ldg16nt(Vp + i * 512);
      SCHED();
    }
#define TR_STEP(HALF, CTRL, BIT)                                                          \
  _Pragma("unroll") for (int h = 0; h < (HALF); ++h) {                                    \
    const float lo_ = x[h], hi_ = x[h + (HALF)];                                          \
    const float t1_ = lo_ + dpp<CTRL>(lo_), t2_ = hi_ + dpp<CTRL>(hi_);                   \
    x[h] = (c & (BIT)) ? t2_ : t1_;                                                       \
  }
    TR_STEP(8, ROR8, 8) TR_STEP(4, XOR7, 4) TR_STEP(2, XOR2, 2) TR_STEP(1, XOR1, 1)
    sc[G] = x[0] * C2;
  }
  float m = fmaxf(sc[0], sc[1]);
  m = fmaxf(m, dpp<XOR2>(m)); m = fmaxf(m, dpp<ROR4>(m)); m = fmaxf(m, dpp<ROR8>(m));
  m = x32_max(x16_max(m));
  const float p0 = ex2(sc[0] - m), p1 = ex2(sc[1] - m);
  float l = p0 + p1;
  l += dpp<XOR2>(l); l += dpp<ROR4>(l); l += dpp<ROR8>(l);
  l = x32_sum(x16_sum(l));
  pw[r * 32 + c] = p0;
  pw[r * 32 + 16 + c] = p1;
  float a0[8], a1[8];
#pragma unroll
  for (int e = 0; e < 8; ++e) { a0[e] = 0.f; a1[e] = 0.f; }
#pragma unroll
  for (int i2 = 0; i2 < 8; ++i2) {
    const f32x4 pp = *(const f32x4*)(pw + r * 32 + i2 * 4);
#pragma unroll
    for (int ii = 0; ii < 2; ++ii) {
      const int i = 2 * i2 + ii;
      const float pa = pp[2 * ii], pb = pp[2 * ii + 1];
#pragma unroll
      for (int e = 0; e < 8; ++e) {
        const float vf = (e & 1) ? bfhi(kv[i][e >> 1]) : bflo(kv[i][e >> 1]);
        a0[e] = fmaf(pa, vf, a0[e]); a1[e] = fmaf(pb, vf, a1[e]);
      }
    }
  }
  STAMP(3);
#pragma unroll
  for (int e = 0; e < 8; ++e) { a0[e] = x32_sum(x16_sum(a0[e])); a1[e] = x32_sum(x16_sum(a1[e])); }
  if (r == 0) {
    float* w = mrg + wave * 256 + c * 8;
    *(f32x4*)(w) = f32x4{a0[0], a0[1], a0[2], a0[3]};
    *(f32x4*)(w + 4) = f32x4{a0[4], a0[5], a0[6], a0[7]};
    *(f32x4*)(w + 128) = f32x4{a1[0], a1[1], a1[2], a1[3]};
    *(f32x4*)(w + 132) = f32x4{a1[4], a1[5], a1[6], a1[7]};
    if (c < 2) *(f32x2*)(mls + wave * 4 + c * 2) = f32x2{m, l};
  }
  BAR();
  if (t < 256) {
    const int g = t >> 7, d = t & 127;
    float mv[16], M = -INFINITY;
#pragma unroll
    for (int v = 0; v < 16; ++v) { mv[v] = mls[v * 4 + g * 2]; M = fmaxf(M, mv[v]); }
    float L = 0.f, A = 0.f;
#pragma unroll
    for (int v = 0; v < 16; ++v) {
      const float w = ex2(mv[v] - M);
      L = fmaf(w, mls[v * 4 + g * 2 + 1], L);
      A = fmaf(w, mrg[v * 256 + t], A);
    }
    const u32 tag32 = (u32)tag;
    const u32 slot = WS_AG + (u32)((((hkv * 2 + g) * 2 + (d >> 6)) * 8 + b * 4 + s) * 66 * 8);
    st8_sc1(p.ws, slot + (d & 63) * 8, ((u64)tag32 << 32) | __float_as_uint(A));
    if ((d & 63) < 2) st8_sc1(p.ws, slot + (64 + (d & 1)) * 8, ((u64)tag32 << 32) | __float_as_uint((d & 1) ? L : M));
  }
  STAMP(4);
}

DI void role_F2(const Params& p, unsigned char* smem, __amdgpu_buffer_rsrc_t rs, u64 epoch, int j) {
  const int t = threadIdx.x, lane = t & 63, wave = __builtin_amdgcn_readfirstlane(t >> 6);
  const int b = j & 1, s = j >> 1;
  float* red = (float*)(smem + QOFF);
  const u32 lds0 = (u32)(size_t)smem;
  u64 tag;
  if (wave < 9) {
    Gemv1 g;
    gemv1_issue(g, p.wqA + 64 * j, p.x, t);
    tag = tag_of(epoch);
    const u32 ntag = ~(u32)tag;
    STAMP(8);
    st_sc1(rs, og_off(0, j, t), u32x4{0u, ntag, 0u, ntag});
    gemv1_finish(g, red, wave, lane);
    STAMP(9);
    DRAIN();
  } else {
    if (wave >= 12) {
      const int w4 = wave - 12;
      const u32 rl = lane >> 4, pos = lane & 15;
#pragma unroll 4
      for (int it = 0; it < 32; ++it) {
        const int n = it * 4 + w4;
        const u32 row = 4 * (n >> 2) + rl, cb = n & 3;
        const u32 ch = pos ^ (((row & 3) << 2) | ((row >> 2) & 3));
        const u16* src = p.ckv + ((size_t)(b * 4096 + s * 128 + row) * 512 + cb * 128 + ch * 8);
        glds16(src, lds0 + cb * 32768 + (n >> 2) * 1024);
        if (it >= 3 && it < 8) asm volatile("s_waitcnt vmcnt(3)" ::: "memory");
      }
    }
    tag = tag_of(epoch);
  }
  STAMP(1);
  BAR();
  if (wave == 0) {
    if (t < 16) {
      const u32x4 q = gemv1_sum(red, t);
      st_sc1(rs, WS_QA + (u32)(((t >> 3) * 4096 + 64 * j + (t & 7) * 8) * 2), q);
    }
    DRAIN();
    if (lane == 0) st8_sc1(p.ws, WS_F2FLAG + j * 8, tag);
    STAMP(2);
  } else if (wave == 9) {
    poll_flags(p.ws, WS_AFLAG + ((j >> 2) + 16 * (lane & 7)) * 8, lane < 8, tag);
    poll_flags(p.ws, WS_F1FLAG + lane * 8, true, tag);
    STAMP(2);
  }
  BAR();
  {
#pragma unroll
    for (int u = 0; u < 2; ++u) {
      const int n = t + 1024 * u;
      const u32x4 v0 = ld_sc1(rs, WS_QABS + (u32)((b * 2 + 0) * 32768 + n * 16));
      const u32x4 v1 = ld_sc1(rs, WS_QABS + (u32)((b * 2 + 1) * 32768 + n * 16));
      u32x4 o;
#pragma unroll
      for (int e = 0; e < 4; ++e) o[e] = pk2(bflo(v0[e]) + bflo(v1[e]), bfhi(v0[e]) + bfhi(v1[e]));
      const u32 h = n >> 6, c8 = n & 63;
      *(u32x4*)(smem + QOFF + (c8 >> 4) * 8192 + off_b(h, c8 & 15)) = o;
    }
    DRAIN();
  }
  STAMP(3);
  BAR();
  u32x4 wo[8];
  const int l31 = lane & 31, hf = lane >> 5;
  float* stat = (float*)(smem + QOFF + 8192);
  f32x16 sacc;
  float Mh = 0.f;
  if (wave < 4) {
#pragma unroll
    for (int i = 0; i < 16; ++i) sacc[i] = 0.f;
#pragma unroll
    for (int ks = 0; ks < 32; ++ks) {
      const u32 cb = ks >> 3, ss = ks & 7;
      const u32x4 av = *(const u32x4*)(smem + cb * 32768 + off_b(32 * wave + l31, 2 * ss + hf));
      const u32x4 bv = *(const u32x4*)(smem + QOFF + cb * 8192 + off_b(l31, 2 * ss + hf));
      sacc = __builtin_amdgcn_mfma_f32_32x32x16_bf16(__builtin_bit_cast(bf16x8, av), __builtin_bit_cast(bf16x8, bv), sacc, 0, 0, 0);
    }
    float mx = -INFINITY;
#pragma unroll
    for (int i = 0; i < 16; ++i) { sacc[i] *= C2; mx = fmaxf(mx, sacc[i]); }
    Mh = x32_max(mx);
  } else if (wave >= 7) {
    oproj_issue(wo, p.woA + (size_t)(64 * j) * 576, t - 448);
  }
  BAR();
  if (wave < 4 && lane < 32) stat[wave * 32 + lane] = Mh;
  BAR();
  if (wave < 4) {
    Mh = fmaxf(fmaxf(stat[l31], stat[32 + l31]), fmaxf(stat[64 + l31], stat[96 + l31]));
    float ls = 0.f;
#pragma unroll
    for (int i = 0; i < 16; ++i) { sacc[i] = ex2(sacc[i] - Mh); ls += sacc[i]; }
    ls = x32_sum(ls);
    if (lane < 32) stat[128 + wave * 32 + lane] = ls;
#pragma unroll
    for (int g = 0; g < 4; ++g) {
      const u32x2 pv = {pk2(sacc[4 * g], sacc[4 * g + 1]), pk2(sacc[4 * g + 2], sacc[4 * g + 3])};
      *(u32x2*)(smem + QOFF + off_b(l31, 4 * wave + g) + 8 * hf) = pv;
    }
  }
  BAR();
  const float Lh = (stat[128 + l31] + stat[160 + l31]) + (stat[192 + l31] + stat[224 + l31]);
  {
    f32x16 oacc;
#pragma unroll
    for (int i = 0; i < 16; ++i) oacc[i] = 0.f;
    const u32 cb = wave >> 2, ct = wave & 3;
    const u32 blk = (lane >> 4) & 1, q4 = (lane & 15) >> 2, p4 = lane & 3;
#pragma unroll
    for (int ks = 0; ks < 8; ++ks) {
      const u32 a0 = cb * 32768 + off_b(16 * ks + 8 * hf + q4, 4 * ct + 2 * blk + (p4 >> 1)) + 8 * (p4 & 1);
      const u32 a1 = cb * 32768 + off_b(16 * ks + 8 * hf + 4 + q4, 4 * ct + 2 * blk + (p4 >> 1)) + 8 * (p4 & 1);
      const s16x4 lo = __builtin_amdgcn_ds_read_tr16_b64_v4i16((LAS s16x4*)(smem + a0));
      const s16x4 hi = __builtin_amdgcn_ds_read_tr16_b64_v4i16((LAS s16x4*)(smem + a1));
      const auto av = __builtin_shufflevector(lo, hi, 0, 1, 2, 3, 4, 5, 6, 7);
      const u32x4 bv = *(const u32x4*)(smem + QOFF + off_b(l31, 2 * ks + hf));
      oacc = __builtin_amdgcn_mfma_f32_32x32x16_bf16(__builtin_bit_cast(bf16x8, av), __builtin_bit_cast(bf16x8, bv), oacc, 0, 0, 0);
    }
    const float li = 1.f / Lh;
    BAR();
#pragma unroll
    for (int g = 0; g < 4; ++g) {
      const u32x2 ov = {pk2(oacc[4 * g] * li, oacc[4 * g + 1] * li), pk2(oacc[4 * g + 2] * li, oacc[4 * g + 3] * li)};
      *(u32x2*)(smem + l31 * 1040 + 64 * wave + 16 * g + 8 * hf) = ov;
    }
  }
  BAR();
#pragma unroll
  for (int u = 0; u < 2; ++u) {
    const int h = 2 * wave + u;
    const u32x4 v = *(const u32x4*)(smem + h * 1040 + lane * 16);
    st_sc1(rs, WS_OLAT + (u32)(((h * 2 + b) * 32 + s) * 1024 + lane * 16), v);
  }
  if (t < 32) st8_sc1(p.ws, WS_ML + (u32)(((t * 2 + b) * 32 + s) * 8), ((u64)__float_as_uint(Lh) << 32) | __float_as_uint(Mh));
  DRAIN();
  BAR();
  if (t == 0) st8_sc1(p.ws, WS_F2FLAG2 + j * 8, tag);
  STAMP(4);
  const u32 tag32 = (u32)tag;
  float* ag = (float*)smem;
  float* zt = (float*)(smem + 4096);
  float* red2 = (float*)(smem + 8192);
  if (t < 264) {
    const f32x2 v = spin_pair(rs, WS_AG + (u32)(j * 8 * 66 * 8 + t * 16), tag32);
    *(f32x2*)(ag + 2 * t) = v;
  }
  STAMP(5);
  BAR();
  if (t < 128) {
    const int bb = t >> 6, d = t & 63;
    const float* a = ag + bb * 4 * 66;
    const float m0 = a[64], m1 = a[66 + 64], m2 = a[132 + 64], m3 = a[198 + 64];
    const float M = fmaxf(fmaxf(m0, m1), fmaxf(m2, m3));
    const float w0 = ex2(m0 - M), w1 = ex2(m1 - M), w2 = ex2(m2 - M), w3 = ex2(m3 - M);
    const float L = w0 * a[65] + w1 * a[66 + 65] + w2 * a[132 + 65] + w3 * a[198 + 65];
    const float z = (w0 * a[d] + w1 * a[66 + d] + w2 * a[132 + d] + w3 * a[198 + d]) / L;
    zt[(d & 7) * 16 + (d >> 3) * 2 + bb] = z;
  }
  BAR();
  if (wave >= 7) oproj_compute(wo, zt, red2, t - 448);
  BAR();
  if (t < 576) oproj_publish(red2, rs, 0, j, tag32, t);
  STAMP(6);
}

DI void role_F1(const Params& p, unsigned char* smem, __amdgpu_buffer_rsrc_t rs, u64 epoch, int w) {
  const int t = threadIdx.x, lane = t & 63, wave = __builtin_amdgcn_readfirstlane(t >> 6);
  const int h = w & 31, dh = w >> 5;
  const int col0 = h * 128 + dh * 64;
  float* red = (float*)smem;
  unsigned char* qsh = smem + 8192;
  float* gsh = (float*)(smem + 8192 + 256);
  float* qas = (float*)(smem + 16384);
  float* zt = (float*)(smem + 24576);
  float* fin = (float*)(smem + 28672);
  float* red2 = (float*)(smem + 40960);
  u64 tag;
  u32x4 uk[4];
  const int ch = t & 7;
  if (wave < 9) {
    Gemv1 g;
    gemv1_issue(g, p.wqB + col0, p.x, t);
#pragma unroll
    for (int k = 0; k < 4; ++k) uk[k] = ldg16nt(p.wuk + (size_t)((t >> 3) + 128 * k) * 4096 + col0 + 8 * ch);
    tag = tag_of(epoch);
    const u32 ntag = ~(u32)tag;
    STAMP(8);
    st_sc1(rs, og_off(1, w, t), u32x4{0u, ntag, 0u, ntag});
    gemv1_finish(g, red, wave, lane);
    STAMP(9);
  } else {
#pragma unroll
    for (int k = 0; k < 4; ++k) uk[k] = ldg16nt(p.wuk + (size_t)((t >> 3) + 128 * k) * 4096 + col0 + 8 * ch);
    tag = tag_of(epoch);
    if (wave == 15) {
      const u16* xr = p.x + (w >> 5) * 576;
      float sg = 0.f;
#pragma unroll
      for (int k = 0; k < 9; ++k) {
        const int mm = lane + 64 * k;
        sg = fmaf(__uint_as_float((u32)xr[mm] << 16), __uint_as_float((u32)p.gw[mm] << 16), sg);
      }
      sg += dpp<XOR1>(sg); sg += dpp<XOR2>(sg); sg += dpp<ROR4>(sg); sg += dpp<ROR8>(sg);
      sg = x32_sum(x16_sum(sg));
      if (lane == 0) gsh[0] = 1.f / (1.f + __expf(-sg));
    }
  }
  BAR();
  if (t < 16) *(u32x4*)(qsh + t * 16) = gemv1_sum(red, t);
  BAR();
  {
    const u32x4 q0 = *(const u32x4*)(qsh + ch * 16), q1 = *(const u32x4*)(qsh + 128 + ch * 16);
    float x[8];
#pragma unroll
    for (int k = 0; k < 4; ++k) { x[2 * k] = dot8(uk[k], q0); x[2 * k + 1] = dot8(uk[k], q1); }
    const int c = ch;
    TR_STEP(4, XOR7, 4) TR_STEP(2, XOR2, 2) TR_STEP(1, XOR1, 1)
    qas[(ch & 1) * 512 + (t >> 3) + 128 * (ch >> 1)] = x[0];
  }
  STAMP(10);
  BAR();
  if (t < 128) {
    const f32x4 v0 = *(const f32x4*)(qas + t * 8), v1 = *(const f32x4*)(qas + t * 8 + 4);
    const u32x4 o = {pk2(v0[0], v0[1]), pk2(v0[2], v0[3]), pk2(v1[0], v1[1]), pk2(v1[2], v1[3])};
    st_sc1(rs, WS_QABS + (u32)((((t >> 6) * 2 + dh) * 32 + h) * 1024 + (t & 63) * 16), o);
  }
  DRAIN();
  BAR();
  if (t == 0) st8_sc1(p.ws, WS_F1FLAG + w * 8, tag);
  STAMP(1);
  u32x4 uv[4], wo[8];
#pragma unroll
  for (int k = 0; k < 4; ++k) uv[k] = ldg16nt(p.wuv + (size_t)(4 * (t >> 3) + k) * 4096 + col0 + 8 * ch);
  if (wave >= 7) oproj_issue(wo, p.woB + (size_t)col0 * 576, t - 448);
  if (wave == 0) {
    poll_flags(p.ws, WS_F1FLAG + lane * 8, true, tag);
    poll_flags(p.ws, WS_F2FLAG2 + lane * 8, true, tag);
  }
  STAMP(2);
  BAR();
  {
    const int bb = wave >> 3, sg = wave & 7;
    u32x4 pv[4];
#pragma unroll
    for (int u = 0; u < 4; ++u) pv[u] = ld_sc1(rs, WS_OLAT + (u32)(((h * 2 + bb) * 32 + 4 * sg + u) * 1024 + lane * 16));
    const u64 ml = ld8_sc1(p.ws, WS_ML + (u32)(((h * 2 + bb) * 32 + (lane & 31)) * 8));
    const float ms = __uint_as_float((u32)ml), ls = __uint_as_float((u32)(ml >> 32));
    float M = ms;
    M = fmaxf(M, dpp<XOR1>(M)); M = fmaxf(M, dpp<XOR2>(M)); M = fmaxf(M, dpp<ROR4>(M)); M = fmaxf(M, dpp<ROR8>(M));
    M = x16_max(M);
    const float wl = ls * ex2(ms - M);
    float L = wl;
    L += dpp<XOR1>(L); L += dpp<XOR2>(L); L += dpp<ROR4>(L); L += dpp<ROR8>(L);
    L = x16_sum(L);
    const float wn = wl / L;
    float a[8];
#pragma unroll
    for (int e = 0; e < 8; ++e) a[e] = 0.f;
#pragma unroll
    for (int u = 0; u < 4; ++u) {
      const float wu = __shfl(wn, 4 * sg + u);
#pragma unroll
      for (int e = 0; e < 8; ++e) a[e] = fmaf(wu, (e & 1) ? bfhi(pv[u][e >> 1]) : bflo(pv[u][e >> 1]), a[e]);
    }
    float* r = red2 + (bb * 8 + sg) * 512 + lane * 8;
    *(f32x4*)(r) = f32x4{a[0], a[1], a[2], a[3]};
    *(f32x4*)(r + 4) = f32x4{a[4], a[5], a[6], a[7]};
  }
  STAMP(3);
  BAR();
  {
    const float* r = red2 + (t >> 9) * 4096 + (t & 511);
    float sum = 0.f;
#pragma unroll
    for (int sg = 0; sg < 8; ++sg) sum += r[sg * 512];
    qas[t] = sum;
  }
  BAR();
  {
    const f32x4 o0 = *(const f32x4*)(qas + 4 * (t >> 3)), o1 = *(const f32x4*)(qas + 512 + 4 * (t >> 3));
    float a0[8], a1[8];
#pragma unroll
    for (int e = 0; e < 8; ++e) { a0[e] = 0.f; a1[e] = 0.f; }
#pragma unroll
    for (int k = 0; k < 4; ++k)
#pragma unroll
      for (int e = 0; e < 8; ++e) {
        const float wf = (e & 1) ? bfhi(uv[k][e >> 1]) : bflo(uv[k][e >> 1]);
        a0[e] = fmaf(o0[k], wf, a0[e]); a1[e] = fmaf(o1[k], wf, a1[e]);
      }
#pragma unroll
    for (int e = 0; e < 8; ++e) {
      a0[e] += dpp<ROR8>(a0[e]); a0[e] = x32_sum(x16_sum(a0[e]));
      a1[e] += dpp<ROR8>(a1[e]); a1[e] = x32_sum(x16_sum(a1[e]));
    }
    if (lane < 8) {
      float* r = red + wave * 128 + lane * 8;
      *(f32x4*)(r) = f32x4{a0[0], a0[1], a0[2], a0[3]};
      *(f32x4*)(r + 4) = f32x4{a0[4], a0[5], a0[6], a0[7]};
      *(f32x4*)(r + 64) = f32x4{a1[0], a1[1], a1[2], a1[3]};
      *(f32x4*)(r + 68) = f32x4{a1[4], a1[5], a1[6], a1[7]};
    }
  }
  BAR();
  if (t < 128) {
    float sum = 0.f;
#pragma unroll
    for (int v = 0; v < 16; ++v) sum += red[v * 128 + t];
    const int d = t & 63;
    zt[(d & 7) * 16 + (d >> 3) * 2 + (t >> 6)] = sum;
  }
  BAR();
  if (wave >= 7) oproj_compute(wo, zt, red2, t - 448);
  BAR();
  const u32 tag32 = (u32)tag;
  if (t < 576) {
    oproj_publish(red2, rs, 1, w, tag32, t);
    STAMP(4);
    const f32x2 vb = spin_pair(rs, WS_OG + (u32)((64 + w) * 64 * 18 * 8 + t * 16), tag32);
    STAMP(5);
    const f32x2 va = spin_pair(rs, WS_OG + (u32)(w * 64 * 18 * 8 + t * 16), tag32);
    const float g = gsh[0];
    *(f32x2*)(fin + 2 * t) = f32x2{g * va[0] + (1.f - g) * vb[0], g * va[1] + (1.f - g) * vb[1]};
  }
  STAMP(6);
  BAR();
  if (t < 192) {
    const int o = t >> 3, k = t & 7;
    float sum = 0.f;
    if (o < 18) {
#pragma unroll
      for (int u = 0; u < 8; ++u) sum += fin[(k * 8 + u) * 18 + o];
    }
    sum += dpp<XOR1>(sum); sum += dpp<XOR2>(sum); sum += dpp<XOR7>(sum);
    if (k == 0 && o < 18) {
      const __bf16 r = (__bf16)sum;
      p.out[w * 18 + o] = __builtin_bit_cast(u16, r);
    }
    if (w == 0 && t == 0) st8_sc1(p.ws, WS_EPOCH, tag);
  }
  STAMP(7);
}

__global__ void __launch_bounds__(1024, 4) fused_kernel(Params p) {
  __shared__ __attribute__((aligned(16))) unsigned char smem[LDS_BYTES];
  const u64 epoch = *(const __attribute__((address_space(4))) u64*)(p.ws + WS_EPOCH);
  STAMP(0);
  const __amdgpu_buffer_rsrc_t rs = __builtin_amdgcn_make_buffer_rsrc(p.ws, 0, WS_END, 0x00020000);
  const int w = blockIdx.x;
  if (w < 64) role_F1(p, smem, rs, epoch, w);
  else if (w < 128) role_F2(p, smem, rs, epoch, w - 64);
  else role_A(p, smem, rs, epoch, w - 128);
}

}

extern "C" void kernel_launch(void* const* d_in, const int* in_sizes, int n_in,
                              void* d_out, int out_size, void* d_ws, size_t ws_size,
                              hipStream_t stream) {
  Params p;
  p.x = (const u16*)d_in[0];
  p.K = (const u16*)d_in[1];
  p.V = (const u16*)d_in[2];
  p.wqA = (const u16*)d_in[3];
  p.woA = (const u16*)d_in[4];
  p.ckv = (const u16*)d_in[6];
  p.wqB = (const u16*)d_in[7];
  p.wuk = (const u16*)d_in[8];
  p.wuv = (const u16*)d_in[9];
  p.woB = (const u16*)d_in[10];
  p.gw = (const u16*)d_in[11];
  p.out = (u16*)d_out;
  p.ws = (unsigned char*)d_ws;
  hipLaunchKernelGGL(fused_kernel, dim3(256), dim3(1024), 0, stream, p);
}
```

```cpp
#include <hip/hip_runtime.h>
#include <hip/hip_bf16.h>
#include <stdint.h>

namespace {

typedef unsigned short u16;
typedef unsigned int u32;
typedef unsigned long long u64;
typedef u32 u32x4 __attribute__((ext_vector_type(4)));
typedef u32 u32x2 __attribute__((ext_vector_type(2)));
typedef float f32x4 __attribute__((ext_vector_type(4)));
typedef float f32x2 __attribute__((ext_vector_type(2)));
typedef float f32x16 __attribute__((ext_vector_type(16)));
typedef short s16x4 __attribute__((ext_vector_type(4)));
typedef __bf16 bf16x2 __attribute__((ext_vector_type(2)));
typedef __bf16 bf16x8 __attribute__((ext_vector_type(8)));
#define GAS __attribute__((address_space(1)))
#define LAS __attribute__((address_space(3)))
#define RLX_AGENT __ATOMIC_RELAXED, __HIP_MEMORY_SCOPE_AGENT
#define DI __device__ __forceinline__

constexpr u32 WS_EPOCH = 0;
constexpr u32 WS_F1FLAG = 256;
constexpr u32 WS_F2FLAG = 1024;
constexpr u32 WS_F2FLAG2 = 2048;
constexpr u32 WS_AFLAG = 3072;
constexpr u32 WS_F1FLAGC = 4096;
constexpr u32 WS_QA = 8192;
constexpr u32 WS_QABS = 32768;
constexpr u32 WS_ML = WS_QABS + 131072;
constexpr u32 WS_OLAT = WS_ML + 16384;
constexpr u32 WS_AG = WS_OLAT + 2097152;
constexpr u32 WS_OG = WS_AG + 64 * 8 * 66 * 8;
constexpr u32 WS_END = WS_OG + 2 * 64 * 576 * 16;

constexpr int LDS_BYTES = 163840;
constexpr u32 QOFF = 131072;
constexpr float C2 = 0.08838834764831845f * 1.4426950408889634f;
constexpr u32 SPIN_CAP = 1u << 18;
#define A_SLOW 3
#define A_SLOWD 1
#define DMA_EARLY 4

#define BAR() asm volatile("s_waitcnt lgkmcnt(0)\n\ts_barrier" ::: "memory")
#define DRAIN() asm volatile("s_waitcnt vmcnt(0)" ::: "memory")
#define SCHED() __builtin_amdgcn_sched_barrier(0)

struct Params {
  const u16 *x, *K, *V, *wqA, *woA, *ckv, *wqB, *wuk, *wuv, *woB, *gw;
  u16* out;
  unsigned char* ws;
};

DI float bflo(u32 v) { return __uint_as_float(v << 16); }
DI float bfhi(u32 v) { return __uint_as_float(v & 0xffff0000u); }
DI u32 pk2(float lo, float hi) {
  bf16x2 r; r[0] = (__bf16)lo; r[1] = (__bf16)hi; return __builtin_bit_cast(u32, r);
}
DI u16 bf1(float v) { const __bf16 r = (__bf16)v; return __builtin_bit_cast(u16, r); }
DI float dot2(u32 a, u32 b, float acc) {
  return __builtin_amdgcn_fdot2_f32_bf16(__builtin_bit_cast(bf16x2, a), __builtin_bit_cast(bf16x2, b), acc, false);
}
DI float dot8(u32x4 a, u32x4 b) {
  float d = dot2(a[0], b[0], 0.f); d = dot2(a[1], b[1], d); d = dot2(a[2], b[2], d); return dot2(a[3], b[3], d);
}
template <int CTRL> DI float dpp(float x) {
  return __builtin_bit_cast(float, __builtin_amdgcn_mov_dpp(__builtin_bit_cast(int, x), CTRL, 0xf, 0xf, true));
}
constexpr int XOR1 = 0xB1, XOR2 = 0x4E, XOR7 = 0x141, ROR8 = 0x128, ROR4 = 0x124;
DI float tr32(float a, float b) {
  auto s = __builtin_amdgcn_permlane32_swap(__float_as_uint(a), __float_as_uint(b), false, false);
  return __uint_as_float(s[0]) + __uint_as_float(s[1]);
}
DI float tr16(float a, float b) {
  auto s = __builtin_amdgcn_permlane16_swap(__float_as_uint(a), __float_as_uint(b), false, false);
  return __uint_as_float(s[0]) + __uint_as_float(s[1]);
}
DI float x16_max(float x) {
  auto s = __builtin_amdgcn_permlane16_swap(__float_as_uint(x), __float_as_uint(x), false, false);
  return fmaxf(__uint_as_float(s[0]), __uint_as_float(s[1]));
}
DI float x32_max(float x) {
  auto s = __builtin_amdgcn_permlane32_swap(__float_as_uint(x), __float_as_uint(x), false, false);
  return fmaxf(__uint_as_float(s[0]), __uint_as_float(s[1]));
}
DI float x16_sum(float x) { return tr16(x, x); }
DI float x32_sum(float x) { return tr32(x, x); }
DI float ex2(float x) { return __builtin_amdgcn_exp2f(x); }

DI u32x4 ldg16(const u16* p) { return *(const GAS u32x4*)p; }
DI u32x4 ldg16nt(const u16* p) { return __builtin_nontemporal_load((const GAS u32x4*)p); }
DI u32x4 ld_sc1(__amdgpu_buffer_rsrc_t rs, u32 off) { return __builtin_amdgcn_raw_buffer_load_b128(rs, off, 0, 16); }
DI void st_sc1(__amdgpu_buffer_rsrc_t rs, u32 off, u32x4 v) { __builtin_amdgcn_raw_buffer_store_b128(v, rs, off, 0, 16); }
DI u64 ld8_sc1(unsigned char* ws, u32 off) { return __hip_atomic_load((GAS u64*)(ws + off), RLX_AGENT); }
DI void st8_sc1(unsigned char* ws, u32 off, u64 v) { __hip_atomic_store((GAS u64*)(ws + off), v, RLX_AGENT); }

DI u64 mix64(u64 e) {
  u64 z = e + 0x9E3779B97F4A7C15ull;
  z = (z ^ (z >> 30)) * 0xBF58476D1CE4E5B9ull;
  z = (z ^ (z >> 27)) * 0x94D049BB133111EBull;
  return z ^ (z >> 31);
}
DI void poll_flags(unsigned char* ws, u32 off, bool active, u64 tag) {
  for (u32 spins = 0; spins < SPIN_CAP; ++spins) {
    bool ok = true;
    if (active) ok = (ld8_sc1(ws, off) == tag);
    if (__all(ok)) break;
    __builtin_amdgcn_s_sleep(1);
  }
  __builtin_amdgcn_fence(__ATOMIC_ACQUIRE, "wavefront");
}
DI f32x2 spin_pair(__amdgpu_buffer_rsrc_t rs, u32 off, u32 tag32) {
  u32x4 v;
  for (u32 spins = 0; spins < SPIN_CAP; ++spins) {
    asm volatile("" ::: "memory");
    v = ld_sc1(rs, off);
    bool ok = (v[1] == tag32) & (v[3] == tag32);
    if (__all(ok)) break;
  }
  f32x2 r; r[0] = __uint_as_float(v[0]); r[1] = __uint_as_float(v[2]); return r;
}
DI void glds16(const void* gsrc, u32 lds_dst) {
  u32 keep;
  asm volatile("s_mov_b32 %0, m0\n\ts_mov_b32 m0, %2\n\ts_nop 0\n\tglobal_load_lds_dwordx4 %1, off nt\n\ts_mov_b32 m0, %0"
               : "=&s"(keep) : "v"(gsrc), "s"(lds_dst) : "memory");
}
DI u32 off_b(u32 row, u32 ch) { return 256u * row + 16u * (ch ^ (((row & 3) << 2) | ((row >> 2) & 3))); }
DI u32 wsw(u32 row) { return (((row >> 1) & 1) | (((row >> 3) & 1) << 1)) << 1; }
DI f32x4 mfma16(u32x4 a, u32x4 b, f32x4 c) {
  return __builtin_amdgcn_mfma_f32_16x16x32_bf16(__builtin_bit_cast(bf16x8, a), __builtin_bit_cast(bf16x8, b), c, 0, 0, 0);
}
DI f32x16 mfma32(u32x4 a, u32x4 b, f32x16 c) {
  return __builtin_amdgcn_mfma_f32_32x32x16_bf16(__builtin_bit_cast(bf16x8, a), __builtin_bit_cast(bf16x8, b), c, 0, 0, 0);
}
DI u32x4 tr_pair(const unsigned char* smem, u32 a0, u32 a1) {
  const s16x4 lo = __builtin_amdgcn_ds_read_tr16_b64_v4i16((LAS s16x4*)(smem + a0));
  const s16x4 hi = __builtin_amdgcn_ds_read_tr16_b64_v4i16((LAS s16x4*)(smem + a1));
  return __builtin_bit_cast(u32x4, __builtin_shufflevector(lo, hi, 0, 1, 2, 3, 4, 5, 6, 7));
}
DI u32x4 wfrag(const unsigned char* smem, u32 img, u32 stride, u32 ks, u32 ct, int lane) {
  const u32 g = lane >> 4, q = (lane & 15) >> 2, pp = lane & 3;
  const u32 r0 = 32 * ks + 8 * g + q, r1 = r0 + 4;
  const u32 ch = 2 * ct + (pp >> 1);
  return tr_pair(smem, img + r0 * stride + ((ch ^ wsw(r0)) << 4) + 8 * (pp & 1),
                       img + r1 * stride + ((ch ^ wsw(r1)) << 4) + 8 * (pp & 1));
}

#define STAMP(i) do {} while (0)

struct Gemv1 { u32x4 w[8]; u32x4 x0, x1; };
DI void gemv1_issue(Gemv1& g, const u16* W, const u16* x, int t) {
  const int rg = t >> 3, ch = t & 7;
  const u16* wp = W + (size_t)(8 * rg) * 4096 + 8 * ch;
  g.x0 = ldg16(x + 8 * rg);
  g.x1 = ldg16(x + 576 + 8 * rg);
#pragma unroll
  for (int k = 0; k < 8; ++k) g.w[k] = ldg16nt(wp + (size_t)k * 4096);
}
DI void gemv1_finish(Gemv1& g, float* red  , int wave, int lane) {
  float a0[8], a1[8];
#pragma unroll
  for (int e = 0; e < 8; ++e) { a0[e] = 0.f; a1[e] = 0.f; }
#pragma unroll
  for (int k = 0; k < 8; ++k) {
    const float xa = (k & 1) ? bfhi(g.x0[k >> 1]) : bflo(g.x0[k >> 1]);
    const float xb = (k & 1) ? bfhi(g.x1[k >> 1]) : bflo(g.x1[k >> 1]);
#pragma unroll
    for (int e = 0; e < 8; ++e) {
      const float wf = (e & 1) ? bfhi(g.w[k][e >> 1]) : bflo(g.w[k][e >> 1]);
      a0[e] = fmaf(xa, wf, a0[e]); a1[e] = fmaf(xb, wf, a1[e]);
    }
  }
  float v[8], u[4], wv[2];
#pragma unroll
  for (int e = 0; e < 8; ++e) v[e] = tr32(a0[e], a1[e]);
#pragma unroll
  for (int e = 0; e < 4; ++e) u[e] = tr16(v[e], v[e + 4]);
#pragma unroll
  for (int e = 0; e < 2; ++e) {
    const float t1 = u[e] + dpp<ROR8>(u[e]), t2 = u[e + 2] + dpp<ROR8>(u[e + 2]);
    wv[e] = (lane & 8) ? t2 : t1;
  }
  *(f32x2*)(red + wave * 128 + (lane >> 5) * 64 + (lane & 7) * 8 + ((lane >> 3) & 1) * 2 + ((lane >> 4) & 1) * 4) = f32x2{wv[0], wv[1]};
}
DI u32x4 gemv1_sum(const float* red, int t) {
  f32x4 s0 = {0.f, 0.f, 0.f, 0.f}, s1 = {0.f, 0.f, 0.f, 0.f};
  const float* r = red + (t >> 3) * 64 + (t & 7) * 8;
#pragma unroll
  for (int v = 0; v < 9; ++v) { s0 += *(const f32x4*)(r + v * 128); s1 += *(const f32x4*)(r + v * 128 + 4); }
  return u32x4{pk2(s0[0], s0[1]), pk2(s0[2], s0[3]), pk2(s1[0], s1[1]), pk2(s1[2], s1[3])};
}
DI void wo_dma(const u16* W, u32 lds_img, int wv, int lane) {
#pragma unroll 3
  for (int it = 0; it < 9; ++it) {
    const int n = wv * 9 + it;
    const u32 P = n * 1024 + lane * 16, row = P / 1152, cpos = (P - row * 1152) >> 4;
    glds16(W + (size_t)row * 576 + ((cpos ^ wsw(row)) << 3), lds_img + n * 1024);
  }
}
DI void oproj_mfma(const unsigned char* smem, u32 img, u32 zimg, __amdgpu_buffer_rsrc_t rs, u32 slot, u32 tag32, int wave, int lane) {
  u32x4 a[2];
#pragma unroll
  for (int ks = 0; ks < 2; ++ks) {
    a[ks] = u32x4{0u, 0u, 0u, 0u};
    if ((lane & 15) < 4) a[ks] = *(const u32x4*)(smem + zimg + (lane & 15) * 128 + (32 * ks + 8 * (lane >> 4)) * 2);
  }
  const int nt = wave < 4 ? 3 : 2;
  u32x4 bfr[3][2];
#pragma unroll
  for (int i = 0; i < 3; ++i)
    if (i < nt) {
#pragma unroll
      for (int ks = 0; ks < 2; ++ks) bfr[i][ks] = wfrag(smem, img, 1152, ks, wave + 16 * i, lane);
    }
#pragma unroll
  for (int i = 0; i < 3; ++i)
    if (i < nt) {
      f32x4 acc = {0.f, 0.f, 0.f, 0.f};
      acc = mfma16(a[0], bfr[i][0], acc);
      acc = mfma16(a[1], bfr[i][1], acc);
      if (lane < 16)
        st_sc1(rs, slot + (u32)(((wave + 16 * i) * 16 + lane) * 16),
               u32x4{__float_as_uint(acc[0] + acc[2]), tag32, __float_as_uint(acc[1] + acc[3]), tag32});
    }
}
DI void z_store(unsigned char* smem, u32 zimg, int b, int d, float z) {
  const u16 hi = bf1(z);
  const u16 lo = bf1(z - __uint_as_float((u32)hi << 16));
  *(u16*)(smem + zimg + b * 128 + d * 2) = hi;
  *(u16*)(smem + zimg + (2 + b) * 128 + d * 2) = lo;
}

#define TR_STEP(HALF, CTRL, BIT)                                                          \
  _Pragma("unroll") for (int h_ = 0; h_ < (HALF); ++h_) {                                 \
    const float lo_ = x[h_], hi_ = x[h_ + (HALF)];                                        \
    const float t1_ = lo_ + dpp<CTRL>(lo_), t2_ = hi_ + dpp<CTRL>(hi_);                   \
    x[h_] = (c & (BIT)) ? t2_ : t1_;                                                      \
  }

DI void role_A(const Params& p, unsigned char* smem, __amdgpu_buffer_rsrc_t rs, u64 tag, int a) {
  const int t = threadIdx.x, lane = t & 63, wave = __builtin_amdgcn_readfirstlane(t >> 6);
  const int r = lane >> 4, c = lane & 15;
  const int hkv = a & 15, b = (a >> 4) & 1, s = a >> 5;
  const size_t base = ((size_t)(b * 16 + hkv) * 4096 + s * 1024 + wave * 64 + r) * 128 + c * 8;
  const u16* Kp = p.K + base;
  const u16* Vp = p.V + base;
  unsigned char* qs = smem;
  float* pw = (float*)(smem + 1024) + wave * 128;
  float* mrg = (float*)(smem + 1024 + 8192);
  float* mls = mrg + 16 * 256;
  u32x4 kv[16];
  if (wave == 0) {
    const u32 ntag = ~(u32)tag;
#pragma unroll
    for (int k = 0; k < 3; ++k) {
      const int n = lane + 64 * k;
      if (n < 132) {
        const int seg = n / 33, pos = n - seg * 33;
        st_sc1(rs, WS_AG + (u32)((((hkv * 4 + seg) * 8 + b * 4 + s) * 66 + 2 * pos) * 8), u32x4{0u, ntag, 0u, ntag});
      }
    }
    DRAIN();
    if (lane == 0) st8_sc1(p.ws, WS_AFLAG + a * 8, tag);
    poll_flags(p.ws, WS_F2FLAG + (hkv * 4 + (lane & 3)) * 8, lane < 4, tag);
    if (lane < 32) {
      const u32x4 q = ld_sc1(rs, WS_QA + (u32)((b * 4096 + hkv * 256 + lane * 8) * 2));
      *(u32x4*)(qs + lane * 16) = q;
    }
    STAMP(1);
    BAR();
#pragma unroll
    for (int i = 0; i < 16; ++i) kv[i] = ldg16nt(Kp + i * 512);
  } else {
#pragma unroll
    for (int i = 0; i < 16; ++i) {
      kv[i] = ldg16nt(Kp + i * 512);
      if (i < A_SLOW) {
        if (A_SLOWD == 1) asm volatile("s_waitcnt vmcnt(0)" ::: "memory");
        else if (i >= 1) asm volatile("s_waitcnt vmcnt(1)" ::: "memory");
      } else if (i >= 3 && i < 15) asm volatile("s_waitcnt vmcnt(3)" ::: "memory");
    }
    STAMP(1);
    BAR();
  }
  STAMP(2);
  const u32x4 q0 = *(const u32x4*)(qs + c * 16), q1 = *(const u32x4*)(qs + 256 + c * 16);
  float sc[2];
#pragma unroll
  for (int G = 0; G < 2; ++G) {
    float x[16];
#pragma unroll
    for (int ii = 0; ii < 8; ++ii) {
      const int i = 8 * G + ii;
      x[2 * ii] = dot8(kv[i], q0);
      x[2 * ii + 1] = dot8(kv[i], q1);
      SCHED();
      kv[i] = ldg16nt(Vp + i * 512);
      SCHED();
    }
    TR_STEP(8, ROR8, 8) TR_STEP(4, XOR7, 4) TR_STEP(2, XOR2, 2) TR_STEP(1, XOR1, 1)
    sc[G] = x[0] * C2;
  }
  float m = fmaxf(sc[0], sc[1]);
  m = fmaxf(m, dpp<XOR2>(m)); m = fmaxf(m, dpp<ROR4>(m)); m = fmaxf(m, dpp<ROR8>(m));
  m = x32_max(x16_max(m));
  const float p0 = ex2(sc[0] - m), p1 = ex2(sc[1] - m);
  float l = p0 + p1;
  l += dpp<XOR2>(l); l += dpp<ROR4>(l); l += dpp<ROR8>(l);
  l = x32_sum(x16_sum(l));
  pw[r * 32 + c] = p0;
  pw[r * 32 + 16 + c] = p1;
  float a0[8], a1[8];
#pragma unroll
  for (int e = 0; e < 8; ++e) { a0[e] = 0.f; a1[e] = 0.f; }
#pragma unroll
  for (int i2 = 0; i2 < 8; ++i2) {
    const f32x4 pp = *(const f32x4*)(pw + r * 32 + i2 * 4);
#pragma unroll
    for (int ii = 0; ii < 2; ++ii) {
      const int i = 2 * i2 + ii;
      const float pa = pp[2 * ii], pb = pp[2 * ii + 1];
#pragma unroll
      for (int e = 0; e < 8; ++e) {
        const float vf = (e & 1) ? bfhi(kv[i][e >> 1]) : bflo(kv[i][e >> 1]);
        a0[e] = fmaf(pa, vf, a0[e]); a1[e] = fmaf(pb, vf, a1[e]);
      }
    }
  }
  STAMP(3);
  {
    float v[8];
#pragma unroll
    for (int e = 0; e < 8; ++e) v[e] = tr32(a0[e], a1[e]);
    f32x4 u;
#pragma unroll
    for (int e = 0; e < 4; ++e) u[e] = tr16(v[e], v[e + 4]);
    *(f32x4*)(mrg + wave * 256 + (lane >> 5) * 128 + c * 8 + ((lane >> 4) & 1) * 4) = u;
    if (lane < 2) *(f32x2*)(mls + wave * 4 + lane * 2) = f32x2{m, l};
  }
  BAR();
  if (t < 256) {
    const int g = t >> 7, d = t & 127;
    float mv[16], M = -INFINITY;
#pragma unroll
    for (int v = 0; v < 16; ++v) { mv[v] = mls[v * 4 + g * 2]; M = fmaxf(M, mv[v]); }
    float L = 0.f, A = 0.f;
#pragma unroll
    for (int v = 0; v < 16; ++v) {
      const float w = ex2(mv[v] - M);
      L = fmaf(w, mls[v * 4 + g * 2 + 1], L);
      A = fmaf(w, mrg[v * 256 + t], A);
    }
    const u32 tag32 = (u32)tag;
    const u32 slot = WS_AG + (u32)((((hkv * 2 + g) * 2 + (d >> 6)) * 8 + b * 4 + s) * 66 * 8);
    st8_sc1(p.ws, slot + (d & 63) * 8, ((u64)tag32 << 32) | __float_as_uint(A));
    if ((d & 63) < 2) st8_sc1(p.ws, slot + (64 + (d & 1)) * 8, ((u64)tag32 << 32) | __float_as_uint((d & 1) ? L : M));
  }
  STAMP(4);
}

DI void ckv_dma(const Params& p, u32 lds0, int b, int s, int w4, int lane, int it) {
  const int n = it * 4 + w4;
  const u32 row = 4 * (n >> 2) + (lane >> 4), cb = n & 3;
  const u32 ch = (lane & 15) ^ (((row & 3) << 2) | ((row >> 2) & 3));
  glds16(p.ckv + ((size_t)(b * 4096 + s * 128 + row) * 512 + cb * 128 + ch * 8), lds0 + cb * 32768 + (n >> 2) * 1024);
}
DI void role_F2(const Params& p, unsigned char* smem, __amdgpu_buffer_rsrc_t rs, u64 tag, int j) {
  const int t = threadIdx.x, lane = t & 63, wave = __builtin_amdgcn_readfirstlane(t >> 6);
  const int b = j & 1, s = j >> 1;
  float* red = (float*)(smem + QOFF);
  const u32 lds0 = (u32)(size_t)smem;
  const u32 tag32 = (u32)tag;
  const u32 oslot = WS_OG + (u32)(j * 9216);
  if (wave < 9) {
    Gemv1 g;
    gemv1_issue(g, p.wqA + 64 * j, p.x, t);
    gemv1_finish(g, red, wave, lane);
    STAMP(9);
  } else if (wave >= 12) {
    for (int it = 0; it < DMA_EARLY; ++it) {
      ckv_dma(p, lds0, b, s, wave - 12, lane, it);
      asm volatile("s_waitcnt vmcnt(1)" ::: "memory");
    }
  }
  STAMP(1);
  BAR();
  if (wave == 0) {
    if (t < 16) {
      const u32x4 q = gemv1_sum(red, t);
      st_sc1(rs, WS_QA + (u32)(((t >> 3) * 4096 + 64 * j + (t & 7) * 8) * 2), q);
    }
    DRAIN();
    if (lane == 0) st8_sc1(p.ws, WS_F2FLAG + j * 8, tag);
    STAMP(2);
  }
  if (wave < 9) {
    st_sc1(rs, oslot + t * 16, u32x4{0u, ~tag32, 0u, ~tag32});
  } else if (wave == 9) {
    poll_flags(p.ws, WS_AFLAG + ((j >> 2) + 16 * (lane & 7)) * 8, lane < 8, tag);
    poll_flags(p.ws, WS_F1FLAG + lane * 8, true, tag);
    STAMP(2);
  } else if (wave >= 12) {
#pragma unroll 4
    for (int it = DMA_EARLY; it < 32; ++it) ckv_dma(p, lds0, b, s, wave - 12, lane, it);
  }
  BAR();
  {
    u32x4 v0[2], v1[2];
#pragma unroll
    for (int u = 0; u < 2; ++u) {
      const int n = t + 1024 * u;
      v0[u] = ld_sc1(rs, WS_QABS + (u32)((b * 2 + 0) * 32768 + n * 16));
      v1[u] = ld_sc1(rs, WS_QABS + (u32)((b * 2 + 1) * 32768 + n * 16));
    }
#pragma unroll
    for (int u = 0; u < 2; ++u) {
      const int n = t + 1024 * u;
      u32x4 o;
#pragma unroll
      for (int e = 0; e < 4; ++e) o[e] = pk2(bflo(v0[u][e]) + bflo(v1[u][e]), bfhi(v0[u][e]) + bfhi(v1[u][e]));
      const u32 h = n >> 6, c8 = n & 63;
      *(u32x4*)(smem + QOFF + (c8 >> 4) * 8192 + off_b(h, c8 & 15)) = o;
    }
    DRAIN();
  }
  STAMP(3);
  BAR();
  const int l31 = lane & 31, hf = lane >> 5;
  float* stat = (float*)(smem + QOFF + 8192);
  f32x16 sacc;
  float Mh = 0.f;
  if (wave < 4) {
#pragma unroll
    for (int i = 0; i < 16; ++i) sacc[i] = 0.f;
    u32x4 av[2][4], bv[2][4];
#define S_LOAD(BT)                                                                              \
  _Pragma("unroll") for (int u = 0; u < 4; ++u) {                                               \
    const u32 ks = 4 * (BT) + u, cb = ks >> 3, ss = ks & 7;                                      \
    av[(BT) & 1][u] = *(const u32x4*)(smem + cb * 32768 + off_b(32 * wave + l31, 2 * ss + hf));  \
    bv[(BT) & 1][u] = *(const u32x4*)(smem + QOFF + cb * 8192 + off_b(l31, 2 * ss + hf));        \
  }
    S_LOAD(0)
#pragma unroll
    for (int bt = 0; bt < 8; ++bt) {
      if (bt < 7) { S_LOAD(bt + 1) }
      SCHED();
#pragma unroll
      for (int u = 0; u < 4; ++u) sacc = mfma32(av[bt & 1][u], bv[bt & 1][u], sacc);
      SCHED();
    }
    float mx = -INFINITY;
#pragma unroll
    for (int i = 0; i < 16; ++i) { sacc[i] *= C2; mx = fmaxf(mx, sacc[i]); }
    Mh = x32_max(mx);
  }
  STAMP(10);
  BAR();
  if (wave < 4 && lane < 32) stat[wave * 32 + lane] = Mh;
  BAR();
  if (wave < 4) {
    Mh = fmaxf(fmaxf(stat[l31], stat[32 + l31]), fmaxf(stat[64 + l31], stat[96 + l31]));
    float ls = 0.f;
#pragma unroll
    for (int i = 0; i < 16; ++i) { sacc[i] = ex2(sacc[i] - Mh); ls += sacc[i]; }
    ls = x32_sum(ls);
    if (lane < 32) stat[128 + wave * 32 + lane] = ls;
#pragma unroll
    for (int g = 0; g < 4; ++g) {
      const u32x2 pv = {pk2(sacc[4 * g], sacc[4 * g + 1]), pk2(sacc[4 * g + 2], sacc[4 * g + 3])};
      *(u32x2*)(smem + QOFF + off_b(l31, 4 * wave + g) + 8 * hf) = pv;
    }
  }
  BAR();
  STAMP(11);
  const float Lh = (stat[128 + l31] + stat[160 + l31]) + (stat[192 + l31] + stat[224 + l31]);
  {
    f32x16 oacc;
#pragma unroll
    for (int i = 0; i < 16; ++i) oacc[i] = 0.f;
    const u32 cb = wave >> 2, ct = wave & 3;
    const u32 blk = (lane >> 4) & 1, q4 = (lane & 15) >> 2, p4 = lane & 3;
    u32x4 av[8], bv[8];
#pragma unroll
    for (int ks = 0; ks < 8; ++ks) {
      av[ks] = tr_pair(smem, cb * 32768 + off_b(16 * ks + 8 * hf + q4, 4 * ct + 2 * blk + (p4 >> 1)) + 8 * (p4 & 1),
                             cb * 32768 + off_b(16 * ks + 8 * hf + 4 + q4, 4 * ct + 2 * blk + (p4 >> 1)) + 8 * (p4 & 1));
      bv[ks] = *(const u32x4*)(smem + QOFF + off_b(l31, 2 * ks + hf));
    }
    SCHED();
#pragma unroll
    for (int ks = 0; ks < 8; ++ks) oacc = mfma32(av[ks], bv[ks], oacc);
    const float li = 1.f / Lh;
    STAMP(12);
    BAR();
#pragma unroll
    for (int g = 0; g < 4; ++g) {
      const u32x2 ov = {pk2(oacc[4 * g] * li, oacc[4 * g + 1] * li), pk2(oacc[4 * g + 2] * li, oacc[4 * g + 3] * li)};
      *(u32x2*)(smem + 81920 + l31 * 1040 + 64 * wave + 16 * g + 8 * hf) = ov;
    }
  }
  BAR();
  STAMP(13);
#pragma unroll
  for (int u = 0; u < 2; ++u) {
    const int h = 2 * wave + u;
    const u32x4 v = *(const u32x4*)(smem + 81920 + h * 1040 + lane * 16);
    st_sc1(rs, WS_OLAT + (u32)(((h * 2 + b) * 32 + s) * 1024 + lane * 16), v);
  }
  if (t < 32) st8_sc1(p.ws, WS_ML + (u32)(((t * 2 + b) * 32 + s) * 8), ((u64)__float_as_uint(Lh) << 32) | __float_as_uint(Mh));
  STAMP(14);
  DRAIN();
  STAMP(15);
  BAR();
  if (t == 0) st8_sc1(p.ws, WS_F2FLAG2 + j * 8, tag);
  STAMP(4);
  if (wave >= 8) { wo_dma(p.woA + (size_t)(64 * j) * 576, lds0, wave - 8, lane); DRAIN(); }
  float* ag = (float*)(smem + QOFF);
  constexpr u32 ZIMG = QOFF + 4096;
  if (t < 264) {
    const f32x2 v = spin_pair(rs, WS_AG + (u32)(j * 8 * 66 * 8 + t * 16), tag32);
    *(f32x2*)(ag + 2 * t) = v;
  }
  STAMP(5);
  BAR();
  if (t < 128) {
    const int bb = t >> 6, d = t & 63;
    const float* a = ag + bb * 4 * 66;
    const float m0 = a[64], m1 = a[66 + 64], m2 = a[132 + 64], m3 = a[198 + 64];
    const float M = fmaxf(fmaxf(m0, m1), fmaxf(m2, m3));
    const float w0 = ex2(m0 - M), w1 = ex2(m1 - M), w2 = ex2(m2 - M), w3 = ex2(m3 - M);
    const float L = w0 * a[65] + w1 * a[66 + 65] + w2 * a[132 + 65] + w3 * a[198 + 65];
    const float z = (w0 * a[d] + w1 * a[66 + d] + w2 * a[132 + d] + w3 * a[198 + d]) / L;
    z_store(smem, ZIMG, bb, d, z);
  }
  BAR();
  oproj_mfma(smem, 0, ZIMG, rs, oslot, tag32, wave, lane);
  STAMP(6);
}

DI void role_F1(const Params& p, unsigned char* smem, __amdgpu_buffer_rsrc_t rs, u64 tag, int w) {
  const int t = threadIdx.x, lane = t & 63, wave = __builtin_amdgcn_readfirstlane(t >> 6);
  const int h = w & 31, dh = w >> 5;
  const int col0 = h * 128 + dh * 64;
  constexpr u32 UV_OFF = 0, WO_OFF = 65536, AI_OFF = 139264, SM_OFF = 155648;
  constexpr u32 ZIMG = SM_OFF + 2048;
  float* red = (float*)(smem + AI_OFF);
  unsigned char* qsh = smem + AI_OFF + 5120;
  float* qas = (float*)(smem + AI_OFF + 8192);
  float* part = (float*)(smem + SM_OFF);
  float* gsh = (float*)(smem + SM_OFF + 2560);
  float* fin = (float*)(smem + SM_OFF + 3072);
  const u32 lds0 = (u32)(size_t)smem;
  const u32 tag32 = (u32)tag;
  const u32 oslot = WS_OG + (u32)((64 + w) * 9216);
  u32x4 uk[4];
  const int ch = t & 7;
  if (wave < 9) {
    Gemv1 g;
    gemv1_issue(g, p.wqB + col0, p.x, t);
#pragma unroll
    for (int k = 0; k < 4; ++k) uk[k] = ldg16nt(p.wuk + (size_t)((t >> 3) + 128 * k) * 4096 + col0 + 8 * ch);
    STAMP(8);
    gemv1_finish(g, red, wave, lane);
    STAMP(9);
  } else {
#pragma unroll
    for (int k = 0; k < 4; ++k) uk[k] = ldg16nt(p.wuk + (size_t)((t >> 3) + 128 * k) * 4096 + col0 + 8 * ch);
    if (wave >= 14) {
      const u16* xr = p.x + (wave - 14) * 576;
      float sg = 0.f;
#pragma unroll
      for (int k = 0; k < 9; ++k) {
        const int mm = lane + 64 * k;
        sg = fmaf(__uint_as_float((u32)xr[mm] << 16), __uint_as_float((u32)p.gw[mm] << 16), sg);
      }
      sg += dpp<XOR1>(sg); sg += dpp<XOR2>(sg); sg += dpp<ROR4>(sg); sg += dpp<ROR8>(sg);
      sg = x32_sum(x16_sum(sg));
      if (lane == 0) gsh[wave - 14] = 1.f / (1.f + __expf(-sg));
    }
  }
  BAR();
  if (t < 16) *(u32x4*)(qsh + t * 16) = gemv1_sum(red, t);
  BAR();
  {
    const u32x4 q0 = *(const u32x4*)(qsh + ch * 16), q1 = *(const u32x4*)(qsh + 128 + ch * 16);
    float x[8];
#pragma unroll
    for (int k = 0; k < 4; ++k) { x[2 * k] = dot8(uk[k], q0); x[2 * k + 1] = dot8(uk[k], q1); }
    const int c = ch;
    TR_STEP(4, XOR7, 4) TR_STEP(2, XOR2, 2) TR_STEP(1, XOR1, 1)
    qas[(ch & 1) * 512 + (t >> 3) + 128 * (ch >> 1)] = x[0];
  }
  STAMP(10);
  BAR();
  if (wave < 2) {
    const f32x4 v0 = *(const f32x4*)(qas + t * 8), v1 = *(const f32x4*)(qas + t * 8 + 4);
    const u32x4 o = {pk2(v0[0], v0[1]), pk2(v0[2], v0[3]), pk2(v1[0], v1[1]), pk2(v1[2], v1[3])};
    st_sc1(rs, WS_QABS + (u32)((((t >> 6) * 2 + dh) * 32 + h) * 1024 + (t & 63) * 16), o);
    DRAIN();
  }
  BAR();
  if (t == 0) st8_sc1(p.ws, WS_F1FLAG + w * 8, tag);
  STAMP(1);
  if (wave < 8) {
    wo_dma(p.woB + (size_t)col0 * 576, lds0 + WO_OFF, wave, lane);
  } else {
#pragma unroll
    for (int it = 0; it < 8; ++it) {
      const int n = (wave - 8) * 8 + it;
      const u32 row = n * 8 + (lane >> 3);
      glds16(p.wuv + (size_t)row * 4096 + col0 + (((lane & 7) ^ wsw(row)) << 3), lds0 + UV_OFF + n * 1024);
    }
  }
  if (t < 576) st_sc1(rs, oslot + t * 16, u32x4{0u, ~tag32, 0u, ~tag32});
  DRAIN();
  BAR();
  if (wave == 0) {
    if (lane == 0) st8_sc1(p.ws, WS_F1FLAGC + w * 8, tag);
    poll_flags(p.ws, WS_F1FLAGC + lane * 8, true, tag);
    poll_flags(p.ws, WS_F2FLAG2 + lane * 8, true, tag);
  }
  STAMP(2);
  BAR();
  {
    const int bb = wave >> 3, sg = wave & 7;
    u32x4 pv[4];
#pragma unroll
    for (int u = 0; u < 4; ++u) pv[u] = ld_sc1(rs, WS_OLAT + (u32)(((h * 2 + bb) * 32 + 4 * sg + u) * 1024 + lane * 16));
    const u64 ml = ld8_sc1(p.ws, WS_ML + (u32)(((h * 2 + bb) * 32 + (lane & 31)) * 8));
    const float ms = __uint_as_float((u32)ml), ls = __uint_as_float((u32)(ml >> 32));
    float M = ms;
    M = fmaxf(M, dpp<XOR1>(M)); M = fmaxf(M, dpp<XOR2>(M)); M = fmaxf(M, dpp<ROR4>(M)); M = fmaxf(M, dpp<ROR8>(M));
    M = x16_max(M);
    const float wl = ls * ex2(ms - M);
    float L = wl;
    L += dpp<XOR1>(L); L += dpp<XOR2>(L); L += dpp<ROR4>(L); L += dpp<ROR8>(L);
    L = x16_sum(L);
    const float wn = wl / L;
    float a[8];
#pragma unroll
    for (int e = 0; e < 8; ++e) a[e] = 0.f;
#pragma unroll
    for (int u = 0; u < 4; ++u) {
      const float wu = __shfl(wn, 4 * sg + u);
#pragma unroll
      for (int e = 0; e < 8; ++e) a[e] = fmaf(wu, (e & 1) ? bfhi(pv[u][e >> 1]) : bflo(pv[u][e >> 1]), a[e]);
    }
    *(u32x4*)(smem + AI_OFF + (lane >> 4) * 4096 + off_b(wave, lane & 15)) =
        u32x4{pk2(a[0], a[1]), pk2(a[2], a[3]), pk2(a[4], a[5]), pk2(a[6], a[7])};
  }
  STAMP(3);
  BAR();
  {
    const int nt = wave & 3, kq = wave >> 2;
    u32x4 af[4], bf[4];
#pragma unroll
    for (int u = 0; u < 4; ++u) {
      af[u] = *(const u32x4*)(smem + AI_OFF + kq * 4096 + off_b(lane & 15, 4 * u + (lane >> 4)));
      bf[u] = wfrag(smem, UV_OFF, 128, 4 * kq + u, nt, lane);
    }
    f32x4 acc = {0.f, 0.f, 0.f, 0.f};
#pragma unroll
    for (int u = 0; u < 4; ++u) acc = mfma16(af[u], bf[u], acc);
    const float sv = x16_sum((acc[0] + acc[1]) + (acc[2] + acc[3]));
    if ((lane & 16) == 0) part[(kq * 2 + (lane >> 5)) * 64 + nt * 16 + (lane & 15)] = sv;
  }
  STAMP(12);
  BAR();
  if (t < 128) z_store(smem, ZIMG, t >> 6, t & 63, (part[t] + part[128 + t]) + (part[256 + t] + part[384 + t]));
  BAR();
  STAMP(13);
  oproj_mfma(smem, WO_OFF, ZIMG, rs, oslot, tag32, wave, lane);
  STAMP(4);
  if (t < 576) {
    const int pp = t / 9, i = t - pp * 9;
    const u32 off = WS_OG + (u32)(pp * 9216 + (9 * w + i) * 16);
    const f32x2 vb = spin_pair(rs, off + 64 * 9216, tag32);
    STAMP(5);
    const f32x2 va = spin_pair(rs, off, tag32);
    const float g0 = gsh[0], g1 = gsh[1];
    *(f32x2*)(fin + 2 * t) = f32x2{g0 * va[0] + (1.f - g0) * vb[0], g1 * va[1] + (1.f - g1) * vb[1]};
  }
  STAMP(6);
  BAR();
  if (t < 192) {
    const int o = t >> 3, k = t & 7;
    float sum = 0.f;
    if (o < 18) {
#pragma unroll
      for (int u = 0; u < 8; ++u) sum += fin[(k * 8 + u) * 18 + o];
    }
    sum += dpp<XOR1>(sum); sum += dpp<XOR2>(sum); sum += dpp<XOR7>(sum);
    if (k == 0 && o < 18) p.out[(o & 1) * 576 + 9 * w + (o >> 1)] = bf1(sum);
    if (w == 0 && t == 0) st8_sc1(p.ws, WS_EPOCH, tag);
  }
  STAMP(7);
}

__global__ void __launch_bounds__(1024, 4) fused_kernel(Params p) {
  __shared__ __attribute__((aligned(16))) unsigned char smem[LDS_BYTES];
  const u64 epoch = *(const __attribute__((address_space(4))) u64*)(p.ws + WS_EPOCH);
  STAMP(0);
  const u64 tag = mix64(epoch);
  const __amdgpu_buffer_rsrc_t rs = __builtin_amdgcn_make_buffer_rsrc(p.ws, 0, WS_END, 0x00020000);
  const int w = blockIdx.x;
  if (w < 64) role_F1(p, smem, rs, tag, w);
  else if (w < 128) role_F2(p, smem, rs, tag, w - 64);
  else role_A(p, smem, rs, tag, w - 128);
}

}

extern "C" void kernel_launch(void* const* d_in, const int* in_sizes, int n_in,
                              void* d_out, int out_size, void* d_ws, size_t ws_size,
                              hipStream_t stream) {
  Params p;
  p.x = (const u16*)d_in[0];
  p.K = (const u16*)d_in[1];
  p.V = (const u16*)d_in[2];
  p.wqA = (const u16*)d_in[3];
  p.woA = (const u16*)d_in[4];
  p.ckv = (const u16*)d_in[6];
  p.wqB = (const u16*)d_in[7];
  p.wuk = (const u16*)d_in[8];
  p.wuv = (const u16*)d_in[9];
  p.woB = (const u16*)d_in[10];
  p.gw = (const u16*)d_in[11];
  p.out = (u16*)d_out;
  p.ws = (unsigned char*)d_ws;
  hipLaunchKernelGGL(fused_kernel, dim3(256), dim3(1024), 0, stream, p);
}
```

```cpp
#define DBG_STAMPS
#define DBG_PRINT
#include <hip/hip_runtime.h>
#include <hip/hip_bf16.h>
#include <stdint.h>

namespace {

typedef unsigned short u16;
typedef unsigned int u32;
typedef unsigned long long u64;
typedef u32 u32x4 __attribute__((ext_vector_type(4)));
typedef u32 u32x2 __attribute__((ext_vector_type(2)));
typedef float f32x4 __attribute__((ext_vector_type(4)));
typedef float f32x2 __attribute__((ext_vector_type(2)));
typedef float f32x16 __attribute__((ext_vector_type(16)));
typedef short s16x4 __attribute__((ext_vector_type(4)));
typedef __bf16 bf16x2 __attribute__((ext_vector_type(2)));
typedef __bf16 bf16x8 __attribute__((ext_vector_type(8)));
#define GAS __attribute__((address_space(1)))
#define LAS __attribute__((address_space(3)))
#define RLX_AGENT __ATOMIC_RELAXED, __HIP_MEMORY_SCOPE_AGENT
#define DI __device__ __forceinline__

constexpr u32 WS_EPOCH = 0;
constexpr u32 WS_F1FLAG = 256;
constexpr u32 WS_F2FLAG2 = 2048;
constexpr u32 WS_AFLAG = 3072;
constexpr u32 WS_F1FLAGC = 4096;
constexpr u32 WS_QA = 8192;
constexpr u32 WS_QABS = 8192 + 65536;
constexpr u32 WS_ML = WS_QABS + 131072;
constexpr u32 WS_OLAT = WS_ML + 16384;
constexpr u32 WS_AG = WS_OLAT + 2097152;
constexpr u32 WS_OG = WS_AG + 64 * 8 * 66 * 8;
constexpr u32 WS_END = WS_OG + 2 * 64 * 576 * 16;
constexpr u32 WS_DBG = 8u << 20;

constexpr int LDS_BYTES = 163840;
constexpr u32 QOFF = 131072;
constexpr float C2 = 0.08838834764831845f * 1.4426950408889634f;
constexpr u32 SPIN_CAP = 1u << 18;
#define A_SLOW 3
#define A_SLOWD 1
#define DMA_EARLY 4
#define A_DEPTH 4
#define WAITV_(n) asm volatile("s_waitcnt vmcnt(" #n ")" ::: "memory")
#define WAITV(n) WAITV_(n)
#define AD1_2 1
#define AD1_3 2
#define AD1_4 3
#define AD1_5 4
#define AD1_6 5
#define AD1__(d) AD1_##d
#define AD1_(d) AD1__(d)

#define BAR() asm volatile("s_waitcnt lgkmcnt(0)\n\ts_barrier" ::: "memory")
#define DRAIN() asm volatile("s_waitcnt vmcnt(0)" ::: "memory")
#define SCHED() __builtin_amdgcn_sched_barrier(0)

struct Params {
  const u16 *x, *K, *V, *wqA, *woA, *ckv, *wqB, *wuk, *wuv, *woB, *gw;
  u16* out;
  unsigned char* ws;
};

DI float bflo(u32 v) { return __uint_as_float(v << 16); }
DI float bfhi(u32 v) { return __uint_as_float(v & 0xffff0000u); }
DI u32 pk2(float lo, float hi) {
  bf16x2 r; r[0] = (__bf16)lo; r[1] = (__bf16)hi; return __builtin_bit_cast(u32, r);
}
DI u16 bf1(float v) { const __bf16 r = (__bf16)v; return __builtin_bit_cast(u16, r); }
DI float dot2(u32 a, u32 b, float acc) {
  return __builtin_amdgcn_fdot2_f32_bf16(__builtin_bit_cast(bf16x2, a), __builtin_bit_cast(bf16x2, b), acc, false);
}
DI float dot8(u32x4 a, u32x4 b) {
  float d = dot2(a[0], b[0], 0.f); d = dot2(a[1], b[1], d); d = dot2(a[2], b[2], d); return dot2(a[3], b[3], d);
}
template <int CTRL> DI float dpp(float x) {
  return __builtin_bit_cast(float, __builtin_amdgcn_mov_dpp(__builtin_bit_cast(int, x), CTRL, 0xf, 0xf, true));
}
constexpr int XOR1 = 0xB1, XOR2 = 0x4E, XOR7 = 0x141, ROR8 = 0x128, ROR4 = 0x124;
DI float tr32(float a, float b) {
  auto s = __builtin_amdgcn_permlane32_swap(__float_as_uint(a), __float_as_uint(b), false, false);
  return __uint_as_float(s[0]) + __uint_as_float(s[1]);
}
DI float tr16(float a, float b) {
  auto s = __builtin_amdgcn_permlane16_swap(__float_as_uint(a), __float_as_uint(b), false, false);
  return __uint_as_float(s[0]) + __uint_as_float(s[1]);
}
DI float x16_max(float x) {
  auto s = __builtin_amdgcn_permlane16_swap(__float_as_uint(x), __float_as_uint(x), false, false);
  return fmaxf(__uint_as_float(s[0]), __uint_as_float(s[1]));
}
DI float x32_max(float x) {
  auto s = __builtin_amdgcn_permlane32_swap(__float_as_uint(x), __float_as_uint(x), false, false);
  return fmaxf(__uint_as_float(s[0]), __uint_as_float(s[1]));
}
DI float x16_sum(float x) { return tr16(x, x); }
DI float x32_sum(float x) { return tr32(x, x); }
DI float ex2(float x) { return __builtin_amdgcn_exp2f(x); }

DI u32x4 ldg16(const u16* p) { return *(const GAS u32x4*)p; }
DI u32x4 ldg16nt(const u16* p) { return __builtin_nontemporal_load((const GAS u32x4*)p); }
DI u32x4 ld_sc1(__amdgpu_buffer_rsrc_t rs, u32 off) { return __builtin_amdgcn_raw_buffer_load_b128(rs, off, 0, 16); }
DI void st_sc1(__amdgpu_buffer_rsrc_t rs, u32 off, u32x4 v) { __builtin_amdgcn_raw_buffer_store_b128(v, rs, off, 0, 16); }
DI u64 ld8_sc1(unsigned char* ws, u32 off) { return __hip_atomic_load((GAS u64*)(ws + off), RLX_AGENT); }
DI void st8_sc1(unsigned char* ws, u32 off, u64 v) { __hip_atomic_store((GAS u64*)(ws + off), v, RLX_AGENT); }

DI u64 mix64(u64 e) {
  u64 z = e + 0x9E3779B97F4A7C15ull;
  z = (z ^ (z >> 30)) * 0xBF58476D1CE4E5B9ull;
  z = (z ^ (z >> 27)) * 0x94D049BB133111EBull;
  return z ^ (z >> 31);
}
DI void poll_flags(unsigned char* ws, u32 off, bool active, u64 tag) {
  for (u32 spins = 0; spins < SPIN_CAP; ++spins) {
    bool ok = true;
    if (active) ok = (ld8_sc1(ws, off) == tag);
    if (__all(ok)) break;
    __builtin_amdgcn_s_sleep(1);
  }
  __builtin_amdgcn_fence(__ATOMIC_ACQUIRE, "wavefront");
}
DI f32x2 spin_pair(__amdgpu_buffer_rsrc_t rs, u32 off, u32 tag32) {
  u32x4 v;
  for (u32 spins = 0; spins < SPIN_CAP; ++spins) {
    asm volatile("" ::: "memory");
    v = ld_sc1(rs, off);
    bool ok = (v[1] == tag32) & (v[3] == tag32);
    if (__all(ok)) break;
  }
  f32x2 r; r[0] = __uint_as_float(v[0]); r[1] = __uint_as_float(v[2]); return r;
}
DI void glds16(const void* gsrc, u32 lds_dst) {
  u32 keep;
  asm volatile("s_mov_b32 %0, m0\n\ts_mov_b32 m0, %2\n\ts_nop 0\n\tglobal_load_lds_dwordx4 %1, off nt\n\ts_mov_b32 m0, %0"
               : "=&s"(keep) : "v"(gsrc), "s"(lds_dst) : "memory");
}
DI u32 off_b(u32 row, u32 ch) { return 256u * row + 16u * (ch ^ (((row & 3) << 2) | ((row >> 2) & 3))); }
DI u32 wsw(u32 row) { return (((row >> 1) & 1) | (((row >> 3) & 1) << 1)) << 1; }
DI f32x4 mfma16(u32x4 a, u32x4 b, f32x4 c) {
  return __builtin_amdgcn_mfma_f32_16x16x32_bf16(__builtin_bit_cast(bf16x8, a), __builtin_bit_cast(bf16x8, b), c, 0, 0, 0);
}
DI f32x16 mfma32(u32x4 a, u32x4 b, f32x16 c) {
  return __builtin_amdgcn_mfma_f32_32x32x16_bf16(__builtin_bit_cast(bf16x8, a), __builtin_bit_cast(bf16x8, b), c, 0, 0, 0);
}
DI u32x4 tr_pair(const unsigned char* smem, u32 a0, u32 a1) {
  const s16x4 lo = __builtin_amdgcn_ds_read_tr16_b64_v4i16((LAS s16x4*)(smem + a0));
  const s16x4 hi = __builtin_amdgcn_ds_read_tr16_b64_v4i16((LAS s16x4*)(smem + a1));
  return __builtin_bit_cast(u32x4, __builtin_shufflevector(lo, hi, 0, 1, 2, 3, 4, 5, 6, 7));
}
DI u32x4 wfrag(const unsigned char* smem, u32 img, u32 stride, u32 ks, u32 ct, int lane) {
  const u32 g = lane >> 4, q = (lane & 15) >> 2, pp = lane & 3;
  const u32 r0 = 32 * ks + 8 * g + q, r1 = r0 + 4;
  const u32 ch = 2 * ct + (pp >> 1);
  return tr_pair(smem, img + r0 * stride + ((ch ^ wsw(r0)) << 4) + 8 * (pp & 1),
                       img + r1 * stride + ((ch ^ wsw(r1)) << 4) + 8 * (pp & 1));
}

#define STAMP(i) do { if ((threadIdx.x & 63) == 0) ((GAS u64*)(p.ws + WS_DBG))[(blockIdx.x * 16 + (threadIdx.x >> 6)) * 16 + (i)] = __builtin_amdgcn_s_memrealtime(); } while (0)

struct Gemv1 { u32x4 w[8]; u32x4 x0, x1; };
DI void gemv1_issue(Gemv1& g, const u16* W, const u16* x, int t) {
  const int rg = t >> 3, ch = t & 7;
  const u16* wp = W + (size_t)(8 * rg) * 4096 + 8 * ch;
  g.x0 = ldg16(x + 8 * rg);
  g.x1 = ldg16(x + 576 + 8 * rg);
#pragma unroll
  for (int k = 0; k < 8; ++k) g.w[k] = ldg16nt(wp + (size_t)k * 4096);
}
DI void gemv1_finish(Gemv1& g, float* red  , int wave, int lane) {
  float a0[8], a1[8];
#pragma unroll
  for (int e = 0; e < 8; ++e) { a0[e] = 0.f; a1[e] = 0.f; }
#pragma unroll
  for (int k = 0; k < 8; ++k) {
    const float xa = (k & 1) ? bfhi(g.x0[k >> 1]) : bflo(g.x0[k >> 1]);
    const float xb = (k & 1) ? bfhi(g.x1[k >> 1]) : bflo(g.x1[k >> 1]);
#pragma unroll
    for (int e = 0; e < 8; ++e) {
      const float wf = (e & 1) ? bfhi(g.w[k][e >> 1]) : bflo(g.w[k][e >> 1]);
      a0[e] = fmaf(xa, wf, a0[e]); a1[e] = fmaf(xb, wf, a1[e]);
    }
  }
  float v[8], u[4], wv[2];
#pragma unroll
  for (int e = 0; e < 8; ++e) v[e] = tr32(a0[e], a1[e]);
#pragma unroll
  for (int e = 0; e < 4; ++e) u[e] = tr16(v[e], v[e + 4]);
#pragma unroll
  for (int e = 0; e < 2; ++e) {
    const float t1 = u[e] + dpp<ROR8>(u[e]), t2 = u[e + 2] + dpp<ROR8>(u[e + 2]);
    wv[e] = (lane & 8) ? t2 : t1;
  }
  *(f32x2*)(red + wave * 128 + (lane >> 5) * 64 + (lane & 7) * 8 + ((lane >> 3) & 1) * 2 + ((lane >> 4) & 1) * 4) = f32x2{wv[0], wv[1]};
}
DI u32x4 gemv1_sum(const float* red, int t) {
  f32x4 s0 = {0.f, 0.f, 0.f, 0.f}, s1 = {0.f, 0.f, 0.f, 0.f};
  const float* r = red + (t >> 3) * 64 + (t & 7) * 8;
#pragma unroll
  for (int v = 0; v < 9; ++v) { s0 += *(const f32x4*)(r + v * 128); s1 += *(const f32x4*)(r + v * 128 + 4); }
  return u32x4{pk2(s0[0], s0[1]), pk2(s0[2], s0[3]), pk2(s1[0], s1[1]), pk2(s1[2], s1[3])};
}
DI void wo_dma(const u16* W, u32 lds_img, int wv, int lane) {
#pragma unroll 3
  for (int it = 0; it < 9; ++it) {
    const int n = wv * 9 + it;
    const u32 P = n * 1024 + lane * 16, row = P / 1152, cpos = (P - row * 1152) >> 4;
    glds16(W + (size_t)row * 576 + ((cpos ^ wsw(row)) << 3), lds_img + n * 1024);
  }
}
DI void oproj_mfma(const unsigned char* smem, u32 img, u32 zimg, __amdgpu_buffer_rsrc_t rs, u32 slot, u32 tag32, int wave, int lane) {
  u32x4 a[2];
#pragma unroll
  for (int ks = 0; ks < 2; ++ks) {
    a[ks] = u32x4{0u, 0u, 0u, 0u};
    if ((lane & 15) < 4) a[ks] = *(const u32x4*)(smem + zimg + (lane & 15) * 128 + (32 * ks + 8 * (lane >> 4)) * 2);
  }
  const int nt = wave < 4 ? 3 : 2;
  u32x4 bfr[3][2];
#pragma unroll
  for (int i = 0; i < 3; ++i)
    if (i < nt) {
#pragma unroll
      for (int ks = 0; ks < 2; ++ks) bfr[i][ks] = wfrag(smem, img, 1152, ks, wave + 16 * i, lane);
    }
#pragma unroll
  for (int i = 0; i < 3; ++i)
    if (i < nt) {
      f32x4 acc = {0.f, 0.f, 0.f, 0.f};
      acc = mfma16(a[0], bfr[i][0], acc);
      acc = mfma16(a[1], bfr[i][1], acc);
      if (lane < 16)
        st_sc1(rs, slot + (u32)(((wave + 16 * i) * 16 + lane) * 16),
               u32x4{__float_as_uint(acc[0] + acc[2]), tag32, __float_as_uint(acc[1] + acc[3]), tag32});
    }
}
DI void z_store(unsigned char* smem, u32 zimg, int b, int d, float z) {
  const u16 hi = bf1(z);
  const u16 lo = bf1(z - __uint_as_float((u32)hi << 16));
  *(u16*)(smem + zimg + b * 128 + d * 2) = hi;
  *(u16*)(smem + zimg + (2 + b) * 128 + d * 2) = lo;
}

#define TR_STEP(HALF, CTRL, BIT)                                                          \
  _Pragma("unroll") for (int h_ = 0; h_ < (HALF); ++h_) {                                 \
    const float lo_ = x[h_], hi_ = x[h_ + (HALF)];                                        \
    const float t1_ = lo_ + dpp<CTRL>(lo_), t2_ = hi_ + dpp<CTRL>(hi_);                   \
    x[h_] = (c & (BIT)) ? t2_ : t1_;                                                      \
  }

DI void role_A(const Params& p, unsigned char* smem, __amdgpu_buffer_rsrc_t rs, u64 tag, int a) {
  const int t = threadIdx.x, lane = t & 63, wave = __builtin_amdgcn_readfirstlane(t >> 6);
  const int r = lane >> 4, c = lane & 15;
  const int hkv = a & 15, b = (a >> 4) & 1, s = a >> 5;
  const size_t base = ((size_t)(b * 16 + hkv) * 4096 + s * 1024 + wave * 64 + r) * 128 + c * 8;
  const u16* Kp = p.K + base;
  const u16* Vp = p.V + base;
  unsigned char* qs = smem;
  float* pw = (float*)(smem + 1024) + wave * 128;
  float* mrg = (float*)(smem + 1024 + 8192);
  float* mls = mrg + 16 * 256;
  u32x4 kv[16];
  if (wave == 0) {
    const u32 ntag = ~(u32)tag;
#pragma unroll
    for (int k = 0; k < 3; ++k) {
      const int n = lane + 64 * k;
      if (n < 132) {
        const int seg = n / 33, pos = n - seg * 33;
        st_sc1(rs, WS_AG + (u32)((((hkv * 4 + seg) * 8 + b * 4 + s) * 66 + 2 * pos) * 8), u32x4{0u, ntag, 0u, ntag});
      }
    }
    DRAIN();
    if (lane == 0) st8_sc1(p.ws, WS_AFLAG + a * 8, tag);
    {
      const u32 thi = (u32)(tag >> 32), tlo = (u32)tag & 0xffff0000u;
      const u32 off = WS_QA + (u32)((b * 4096 + hkv * 256 + lane * 4) * 8);
      u32x4 g0, g1;
      for (u32 spins = 0; spins < SPIN_CAP; ++spins) {
        asm volatile("" ::: "memory");
        g0 = ld_sc1(rs, off); g1 = ld_sc1(rs, off + 16);
        const bool ok = (g0[1] == thi) & (g0[3] == thi) & (g1[1] == thi) & (g1[3] == thi) &
                        ((g0[0] & 0xffff0000u) == tlo) & ((g0[2] & 0xffff0000u) == tlo) &
                        ((g1[0] & 0xffff0000u) == tlo) & ((g1[2] & 0xffff0000u) == tlo);
        if (__all(ok)) break;
        __builtin_amdgcn_s_sleep(1);
      }
      *(u32x2*)(qs + lane * 8) = u32x2{(g0[0] & 0xffffu) | (g0[2] << 16), (g1[0] & 0xffffu) | (g1[2] << 16)};
    }
    STAMP(1);
    BAR();
#pragma unroll
    for (int i = 0; i < 16; ++i) kv[i] = ldg16nt(Kp + i * 512);
  } else {
#pragma unroll
    for (int i = 0; i < 16; ++i) {
      kv[i] = ldg16nt(Kp + i * 512);
      if (i < A_SLOW) {
        if (A_SLOWD == 1) asm volatile("s_waitcnt vmcnt(0)" ::: "memory");
        else if (i >= 1) asm volatile("s_waitcnt vmcnt(1)" ::: "memory");
      } else if (i >= A_DEPTH - 1 && i < 15) WAITV(AD1_(A_DEPTH));
    }
    STAMP(1);
    BAR();
  }
  STAMP(2);
  const u32x4 q0 = *(const u32x4*)(qs + c * 16), q1 = *(const u32x4*)(qs + 256 + c * 16);
  float sc[2];
#pragma unroll
  for (int G = 0; G < 2; ++G) {
    float x[16];
#pragma unroll
    for (int ii = 0; ii < 8; ++ii) {
      const int i = 8 * G + ii;
      x[2 * ii] = dot8(kv[i], q0);
      x[2 * ii + 1] = dot8(kv[i], q1);
      SCHED();
      kv[i] = ldg16nt(Vp + i * 512);
      SCHED();
    }
    TR_STEP(8, ROR8, 8) TR_STEP(4, XOR7, 4) TR_STEP(2, XOR2, 2) TR_STEP(1, XOR1, 1)
    sc[G] = x[0] * C2;
  }
  float m = fmaxf(sc[0], sc[1]);
  m = fmaxf(m, dpp<XOR2>(m)); m = fmaxf(m, dpp<ROR4>(m)); m = fmaxf(m, dpp<ROR8>(m));
  m = x32_max(x16_max(m));
  const float p0 = ex2(sc[0] - m), p1 = ex2(sc[1] - m);
  float l = p0 + p1;
  l += dpp<XOR2>(l); l += dpp<ROR4>(l); l += dpp<ROR8>(l);
  l = x32_sum(x16_sum(l));
  pw[r * 32 + c] = p0;
  pw[r * 32 + 16 + c] = p1;
  float a0[8], a1[8];
#pragma unroll
  for (int e = 0; e < 8; ++e) { a0[e] = 0.f; a1[e] = 0.f; }
#pragma unroll
  for (int i2 = 0; i2 < 8; ++i2) {
    const f32x4 pp = *(const f32x4*)(pw + r * 32 + i2 * 4);
#pragma unroll
    for (int ii = 0; ii < 2; ++ii) {
      const int i = 2 * i2 + ii;
      const float pa = pp[2 * ii], pb = pp[2 * ii + 1];
#pragma unroll
      for (int e = 0; e < 8; ++e) {
        const float vf = (e & 1) ? bfhi(kv[i][e >> 1]) : bflo(kv[i][e >> 1]);
        a0[e] = fmaf(pa, vf, a0[e]); a1[e] = fmaf(pb, vf, a1[e]);
      }
    }
  }
  STAMP(3);
  {
    float v[8];
#pragma unroll
    for (int e = 0; e < 8; ++e) v[e] = tr32(a0[e], a1[e]);
    f32x4 u;
#pragma unroll
    for (int e = 0; e < 4; ++e) u[e] = tr16(v[e], v[e + 4]);
    *(f32x4*)(mrg + wave * 256 + (lane >> 5) * 128 + c * 8 + ((lane >> 4) & 1) * 4) = u;
    if (lane < 2) *(f32x2*)(mls + wave * 4 + lane * 2) = f32x2{m, l};
  }
  BAR();
  if (t < 256) {
    const int g = t >> 7, d = t & 127;
    float mv[16], M = -INFINITY;
#pragma unroll
    for (int v = 0; v < 16; ++v) { mv[v] = mls[v * 4 + g * 2]; M = fmaxf(M, mv[v]); }
    float L = 0.f, A = 0.f;
#pragma unroll
    for (int v = 0; v < 16; ++v) {
      const float w = ex2(mv[v] - M);
      L = fmaf(w, mls[v * 4 + g * 2 + 1], L);
      A = fmaf(w, mrg[v * 256 + t], A);
    }
    const u32 tag32 = (u32)tag;
    const u32 slot = WS_AG + (u32)((((hkv * 2 + g) * 2 + (d >> 6)) * 8 + b * 4 + s) * 66 * 8);
    st8_sc1(p.ws, slot + (d & 63) * 8, ((u64)tag32 << 32) | __float_as_uint(A));
    if ((d & 63) < 2) st8_sc1(p.ws, slot + (64 + (d & 1)) * 8, ((u64)tag32 << 32) | __float_as_uint((d & 1) ? L : M));
  }
  STAMP(4);
}

DI void ckv_dma(const Params& p, u32 lds0, int b, int s, int w4, int lane, int it) {
  const int n = it * 4 + w4;
  const u32 row = 4 * (n >> 2) + (lane >> 4), cb = n & 3;
  const u32 ch = (lane & 15) ^ (((row & 3) << 2) | ((row >> 2) & 3));
  glds16(p.ckv + ((size_t)(b * 4096 + s * 128 + row) * 512 + cb * 128 + ch * 8), lds0 + cb * 32768 + (n >> 2) * 1024);
}
DI void role_F2(const Params& p, unsigned char* smem, __amdgpu_buffer_rsrc_t rs, u64 tag, int j) {
  const int t = threadIdx.x, lane = t & 63, wave = __builtin_amdgcn_readfirstlane(t >> 6);
  const int b = j & 1, s = j >> 1;
  float* red = (float*)(smem + QOFF);
  const u32 lds0 = (u32)(size_t)smem;
  const u32 tag32 = (u32)tag;
  const u32 oslot = WS_OG + (u32)(j * 9216);
  if (wave < 9) {
    Gemv1 g;
    gemv1_issue(g, p.wqA + 64 * j, p.x, t);
    gemv1_finish(g, red, wave, lane);
    STAMP(9);
  } else if (wave >= 12) {
    for (int it = 0; it < DMA_EARLY; ++it) {
      ckv_dma(p, lds0, b, s, wave - 12, lane, it);
      asm volatile("s_waitcnt vmcnt(1)" ::: "memory");
    }
  }
  STAMP(1);
  BAR();
  if (wave == 0) {
    f32x2 sq = {0.f, 0.f};
#pragma unroll
    for (int v = 0; v < 9; ++v) sq += *(const f32x2*)(red + v * 128 + 2 * lane);
    const u32 thi = (u32)(tag >> 32), tlo = (u32)tag & 0xffff0000u;
    st_sc1(rs, WS_QA + (u32)(((lane >> 5) * 4096 + 64 * j + (lane & 31) * 2) * 8),
           u32x4{tlo | bf1(sq[0]), thi, tlo | bf1(sq[1]), thi});
    STAMP(2);
  }
  if (wave < 9) {
    st_sc1(rs, oslot + t * 16, u32x4{0u, ~tag32, 0u, ~tag32});
  } else if (wave == 9) {
    poll_flags(p.ws, WS_AFLAG + ((j >> 2) + 16 * (lane & 7)) * 8, lane < 8, tag);
    poll_flags(p.ws, WS_F1FLAG + lane * 8, true, tag);
    STAMP(2);
  } else if (wave >= 12) {
#pragma unroll 4
    for (int it = DMA_EARLY; it < 32; ++it) { ckv_dma(p, lds0, b, s, wave - 12, lane, it); WAITV(7); }
  }
  BAR();
  {
    u32x4 v0[2], v1[2];
#pragma unroll
    for (int u = 0; u < 2; ++u) {
      const int n = t + 1024 * u;
      v0[u] = ld_sc1(rs, WS_QABS + (u32)((b * 2 + 0) * 32768 + n * 16));
      v1[u] = ld_sc1(rs, WS_QABS + (u32)((b * 2 + 1) * 32768 + n * 16));
    }
#pragma unroll
    for (int u = 0; u < 2; ++u) {
      const int n = t + 1024 * u;
      u32x4 o;
#pragma unroll
      for (int e = 0; e < 4; ++e) o[e] = pk2(bflo(v0[u][e]) + bflo(v1[u][e]), bfhi(v0[u][e]) + bfhi(v1[u][e]));
      const u32 h = n >> 6, c8 = n & 63;
      *(u32x4*)(smem + QOFF + (c8 >> 4) * 8192 + off_b(h, c8 & 15)) = o;
    }
    DRAIN();
  }
  STAMP(3);
  BAR();
  STAMP(7);
  if (lane == 0) ((GAS u64*)(p.ws + WS_DBG))[(256 * 16 + blockIdx.x * 16 + wave) * 16 + 0] = __builtin_amdgcn_s_memtime();
  const int l31 = lane & 31, hf = lane >> 5;
  float* stat = (float*)(smem + QOFF + 8192);
  f32x16 sacc;
  float Mh = 0.f;
  if (wave < 4) {
#pragma unroll
    for (int i = 0; i < 16; ++i) sacc[i] = 0.f;
    u32x4 av[2][4], bv[2][4];
#define S_LOAD(BT)                                                                              \
  _Pragma("unroll") for (int u = 0; u < 4; ++u) {                                               \
    const u32 ks = 4 * (BT) + u, cb = ks >> 3, ss = ks & 7;                                      \
    av[(BT) & 1][u] = *(const u32x4*)(smem + cb * 32768 + off_b(32 * wave + l31, 2 * ss + hf));  \
    bv[(BT) & 1][u] = *(const u32x4*)(smem + QOFF + cb * 8192 + off_b(l31, 2 * ss + hf));        \
  }
    S_LOAD(0)
#pragma unroll
    for (int bt = 0; bt < 8; ++bt) {
      if (bt < 7) { S_LOAD(bt + 1) }
      SCHED();
#pragma unroll
      for (int u = 0; u < 4; ++u) sacc = mfma32(av[bt & 1][u], bv[bt & 1][u], sacc);
      SCHED();
    }
    float mx = -INFINITY;
#pragma unroll
    for (int i = 0; i < 16; ++i) { sacc[i] *= C2; mx = fmaxf(mx, sacc[i]); }
    Mh = x32_max(mx);
  }
  STAMP(10);
  if (lane == 0) ((GAS u64*)(p.ws + WS_DBG))[(256 * 16 + blockIdx.x * 16 + wave) * 16 + 1] = __builtin_amdgcn_s_memtime();
  BAR();
  if (wave < 4 && lane < 32) stat[wave * 32 + lane] = Mh;
  BAR();
  if (wave < 4) {
    Mh = fmaxf(fmaxf(stat[l31], stat[32 + l31]), fmaxf(stat[64 + l31], stat[96 + l31]));
    float ls = 0.f;
#pragma unroll
    for (int i = 0; i < 16; ++i) { sacc[i] = ex2(sacc[i] - Mh); ls += sacc[i]; }
    ls = x32_sum(ls);
    if (lane < 32) stat[128 + wave * 32 + lane] = ls;
#pragma unroll
    for (int g = 0; g < 4; ++g) {
      const u32x2 pv = {pk2(sacc[4 * g], sacc[4 * g + 1]), pk2(sacc[4 * g + 2], sacc[4 * g + 3])};
      *(u32x2*)(smem + QOFF + off_b(l31, 4 * wave + g) + 8 * hf) = pv;
    }
  }
  BAR();
  STAMP(11);
  const float Lh = (stat[128 + l31] + stat[160 + l31]) + (stat[192 + l31] + stat[224 + l31]);
  {
    f32x16 oacc;
#pragma unroll
    for (int i = 0; i < 16; ++i) oacc[i] = 0.f;
    const u32 cb = wave >> 2, ct = wave & 3;
    const u32 blk = (lane >> 4) & 1, q4 = (lane & 15) >> 2, p4 = lane & 3;
    u32x4 av[8], bv[8];
#pragma unroll
    for (int ks = 0; ks < 8; ++ks) {
      av[ks] = tr_pair(smem, cb * 32768 + off_b(16 * ks + 8 * hf + q4, 4 * ct + 2 * blk + (p4 >> 1)) + 8 * (p4 & 1),
                             cb * 32768 + off_b(16 * ks + 8 * hf + 4 + q4, 4 * ct + 2 * blk + (p4 >> 1)) + 8 * (p4 & 1));
      bv[ks] = *(const u32x4*)(smem + QOFF + off_b(l31, 2 * ks + hf));
    }
    SCHED();
#pragma unroll
    for (int ks = 0; ks < 8; ++ks) oacc = mfma32(av[ks], bv[ks], oacc);
    const float li = 1.f / Lh;
    STAMP(12);
    BAR();
#pragma unroll
    for (int g = 0; g < 4; ++g) {
      const u32x2 ov = {pk2(oacc[4 * g] * li, oacc[4 * g + 1] * li), pk2(oacc[4 * g + 2] * li, oacc[4 * g + 3] * li)};
      *(u32x2*)(smem + 81920 + l31 * 1040 + 64 * wave + 16 * g + 8 * hf) = ov;
    }
  }
  BAR();
  STAMP(13);
#pragma unroll
  for (int u = 0; u < 2; ++u) {
    const int h = 2 * wave + u;
    const u32x4 v = *(const u32x4*)(smem + 81920 + h * 1040 + lane * 16);
    st_sc1(rs, WS_OLAT + (u32)(((h * 2 + b) * 32 + s) * 1024 + lane * 16), v);
  }
  if (t < 32) st8_sc1(p.ws, WS_ML + (u32)(((t * 2 + b) * 32 + s) * 8), ((u64)__float_as_uint(Lh) << 32) | __float_as_uint(Mh));
  STAMP(14);
  DRAIN();
  STAMP(15);
  BAR();
  if (t == 0) st8_sc1(p.ws, WS_F2FLAG2 + j * 8, tag);
  STAMP(4);
  if (wave >= 8) { wo_dma(p.woA + (size_t)(64 * j) * 576, lds0, wave - 8, lane); DRAIN(); }
  float* ag = (float*)(smem + QOFF);
  constexpr u32 ZIMG = QOFF + 4096;
  if (t < 264) {
    const f32x2 v = spin_pair(rs, WS_AG + (u32)(j * 8 * 66 * 8 + t * 16), tag32);
    *(f32x2*)(ag + 2 * t) = v;
  }
  STAMP(5);
  BAR();
  if (t < 128) {
    const int bb = t >> 6, d = t & 63;
    const float* a = ag + bb * 4 * 66;
    const float m0 = a[64], m1 = a[66 + 64], m2 = a[132 + 64], m3 = a[198 + 64];
    const float M = fmaxf(fmaxf(m0, m1), fmaxf(m2, m3));
    const float w0 = ex2(m0 - M), w1 = ex2(m1 - M), w2 = ex2(m2 - M), w3 = ex2(m3 - M);
    const float L = w0 * a[65] + w1 * a[66 + 65] + w2 * a[132 + 65] + w3 * a[198 + 65];
    const float z = (w0 * a[d] + w1 * a[66 + d] + w2 * a[132 + d] + w3 * a[198 + d]) / L;
    z_store(smem, ZIMG, bb, d, z);
  }
  BAR();
  oproj_mfma(smem, 0, ZIMG, rs, oslot, tag32, wave, lane);
  STAMP(6);
}

DI void role_F1(const Params& p, unsigned char* smem, __amdgpu_buffer_rsrc_t rs, u64 tag, int w) {
  const int t = threadIdx.x, lane = t & 63, wave = __builtin_amdgcn_readfirstlane(t >> 6);
  const int h = w & 31, dh = w >> 5;
  const int col0 = h * 128 + dh * 64;
  constexpr u32 UV_OFF = 0, WO_OFF = 65536, AI_OFF = 139264, SM_OFF = 155648;
  constexpr u32 ZIMG = SM_OFF + 2048;
  float* red = (float*)(smem + AI_OFF);
  unsigned char* qsh = smem + AI_OFF + 5120;
  float* qas = (float*)(smem + AI_OFF + 8192);
  float* part = (float*)(smem + SM_OFF);
  float* gsh = (float*)(smem + SM_OFF + 2560);
  float* fin = (float*)(smem + SM_OFF + 3072);
  const u32 lds0 = (u32)(size_t)smem;
  const u32 tag32 = (u32)tag;
  const u32 oslot = WS_OG + (u32)((64 + w) * 9216);
  u32x4 uk[4];
  const int ch = t & 7;
  if (wave < 9) {
    Gemv1 g;
    gemv1_issue(g, p.wqB + col0, p.x, t);
#pragma unroll
    for (int k = 0; k < 4; ++k) uk[k] = ldg16nt(p.wuk + (size_t)((t >> 3) + 128 * k) * 4096 + col0 + 8 * ch);
    STAMP(8);
    gemv1_finish(g, red, wave, lane);
    STAMP(9);
  } else {
#pragma unroll
    for (int k = 0; k < 4; ++k) uk[k] = ldg16nt(p.wuk + (size_t)((t >> 3) + 128 * k) * 4096 + col0 + 8 * ch);
    if (wave >= 14) {
      const u16* xr = p.x + (wave - 14) * 576;
      float sg = 0.f;
#pragma unroll
      for (int k = 0; k < 9; ++k) {
        const int mm = lane + 64 * k;
        sg = fmaf(__uint_as_float((u32)xr[mm] << 16), __uint_as_float((u32)p.gw[mm] << 16), sg);
      }
      sg += dpp<XOR1>(sg); sg += dpp<XOR2>(sg); sg += dpp<ROR4>(sg); sg += dpp<ROR8>(sg);
      sg = x32_sum(x16_sum(sg));
      if (lane == 0) gsh[wave - 14] = 1.f / (1.f + __expf(-sg));
    }
  }
  BAR();
  if (t < 16) *(u32x4*)(qsh + t * 16) = gemv1_sum(red, t);
  BAR();
  {
    const u32x4 q0 = *(const u32x4*)(qsh + ch * 16), q1 = *(const u32x4*)(qsh + 128 + ch * 16);
    float x[8];
#pragma unroll
    for (int k = 0; k < 4; ++k) { x[2 * k] = dot8(uk[k], q0); x[2 * k + 1] = dot8(uk[k], q1); }
    const int c = ch;
    TR_STEP(4, XOR7, 4) TR_STEP(2, XOR2, 2) TR_STEP(1, XOR1, 1)
    qas[(ch & 1) * 512 + (t >> 3) + 128 * (ch >> 1)] = x[0];
  }
  STAMP(10);
  BAR();
  if (wave < 2) {
    const f32x4 v0 = *(const f32x4*)(qas + t * 8), v1 = *(const f32x4*)(qas + t * 8 + 4);
    const u32x4 o = {pk2(v0[0], v0[1]), pk2(v0[2], v0[3]), pk2(v1[0], v1[1]), pk2(v1[2], v1[3])};
    st_sc1(rs, WS_QABS + (u32)((((t >> 6) * 2 + dh) * 32 + h) * 1024 + (t & 63) * 16), o);
    DRAIN();
  }
  BAR();
  if (t == 0) st8_sc1(p.ws, WS_F1FLAG + w * 8, tag);
  STAMP(1);
  if (wave < 8) {
    wo_dma(p.woB + (size_t)col0 * 576, lds0 + WO_OFF, wave, lane);
  } else {
#pragma unroll
    for (int it = 0; it < 8; ++it) {
      const int n = (wave - 8) * 8 + it;
      const u32 row = n * 8 + (lane >> 3);
      glds16(p.wuv + (size_t)row * 4096 + col0 + (((lane & 7) ^ wsw(row)) << 3), lds0 + UV_OFF + n * 1024);
    }
  }
  if (t < 576) st_sc1(rs, oslot + t * 16, u32x4{0u, ~tag32, 0u, ~tag32});
  DRAIN();
  BAR();
  if (wave == 0) {
    if (lane == 0) st8_sc1(p.ws, WS_F1FLAGC + w * 8, tag);
    poll_flags(p.ws, WS_F1FLAGC + lane * 8, true, tag);
    poll_flags(p.ws, WS_F2FLAG2 + lane * 8, true, tag);
  }
  STAMP(2);
  BAR();
  {
    const int bb = wave >> 3, sg = wave & 7;
    u32x4 pv[4];
#pragma unroll
    for (int u = 0; u < 4; ++u) pv[u] = ld_sc1(rs, WS_OLAT + (u32)(((h * 2 + bb) * 32 + 4 * sg + u) * 1024 + lane * 16));
    const u64 ml = ld8_sc1(p.ws, WS_ML + (u32)(((h * 2 + bb) * 32 + (lane & 31)) * 8));
    const float ms = __uint_as_float((u32)ml), ls = __uint_as_float((u32)(ml >> 32));
    float M = ms;
    M = fmaxf(M, dpp<XOR1>(M)); M = fmaxf(M, dpp<XOR2>(M)); M = fmaxf(M, dpp<ROR4>(M)); M = fmaxf(M, dpp<ROR8>(M));
    M = x16_max(M);
    const float wl = ls * ex2(ms - M);
    float L = wl;
    L += dpp<XOR1>(L); L += dpp<XOR2>(L); L += dpp<ROR4>(L); L += dpp<ROR8>(L);
    L = x16_sum(L);
    const float wn = wl / L;
    float a[8];
#pragma unroll
    for (int e = 0; e < 8; ++e) a[e] = 0.f;
#pragma unroll
    for (int u = 0; u < 4; ++u) {
      const float wu = __shfl(wn, 4 * sg + u);
#pragma unroll
      for (int e = 0; e < 8; ++e) a[e] = fmaf(wu, (e & 1) ? bfhi(pv[u][e >> 1]) : bflo(pv[u][e >> 1]), a[e]);
    }
    *(u32x4*)(smem + AI_OFF + (lane >> 4) * 4096 + off_b(wave, lane & 15)) =
        u32x4{pk2(a[0], a[1]), pk2(a[2], a[3]), pk2(a[4], a[5]), pk2(a[6], a[7])};
  }
  STAMP(3);
  BAR();
  {
    const int nt = wave & 3, kq = wave >> 2;
    u32x4 af[4], bf[4];
#pragma unroll
    for (int u = 0; u < 4; ++u) {
      af[u] = *(const u32x4*)(smem + AI_OFF + kq * 4096 + off_b(lane & 15, 4 * u + (lane >> 4)));
      bf[u] = wfrag(smem, UV_OFF, 128, 4 * kq + u, nt, lane);
    }
    f32x4 acc = {0.f, 0.f, 0.f, 0.f};
#pragma unroll
    for (int u = 0; u < 4; ++u) acc = mfma16(af[u], bf[u], acc);
    const float sv = x16_sum((acc[0] + acc[1]) + (acc[2] + acc[3]));
    if ((lane & 16) == 0) part[(kq * 2 + (lane >> 5)) * 64 + nt * 16 + (lane & 15)] = sv;
  }
  STAMP(12);
  BAR();
  if (t < 128) z_store(smem, ZIMG, t >> 6, t & 63, (part[t] + part[128 + t]) + (part[256 + t] + part[384 + t]));
  BAR();
  STAMP(13);
  oproj_mfma(smem, WO_OFF, ZIMG, rs, oslot, tag32, wave, lane);
  STAMP(4);
  if (t < 576) {
    const int pp = t / 9, i = t - pp * 9;
    const u32 off = WS_OG + (u32)(pp * 9216 + (9 * w + i) * 16);
    const f32x2 vb = spin_pair(rs, off + 64 * 9216, tag32);
    STAMP(5);
    const f32x2 va = spin_pair(rs, off, tag32);
    const float g0 = gsh[0], g1 = gsh[1];
    *(f32x2*)(fin + 2 * t) = f32x2{g0 * va[0] + (1.f - g0) * vb[0], g1 * va[1] + (1.f - g1) * vb[1]};
  }
  STAMP(6);
  BAR();
  if (t < 192) {
    const int o = t >> 3, k = t & 7;
    float sum = 0.f;
    if (o < 18) {
#pragma unroll
      for (int u = 0; u < 8; ++u) sum += fin[(k * 8 + u) * 18 + o];
    }
    sum += dpp<XOR1>(sum); sum += dpp<XOR2>(sum); sum += dpp<XOR7>(sum);
    if (k == 0 && o < 18) p.out[(o & 1) * 576 + 9 * w + (o >> 1)] = bf1(sum);
    if (w == 0 && t == 0) st8_sc1(p.ws, WS_EPOCH, tag);
  }
  STAMP(7);
}

__global__ void __launch_bounds__(1024, 4) fused_kernel(Params p) {
  __shared__ __attribute__((aligned(16))) unsigned char smem[LDS_BYTES];
  const u64 epoch = *(const __attribute__((address_space(4))) u64*)(p.ws + WS_EPOCH);
  STAMP(0);
  const u64 tag = mix64(epoch);
  const __amdgpu_buffer_rsrc_t rs = __builtin_amdgcn_make_buffer_rsrc(p.ws, 0, WS_END, 0x00020000);
  const int w = blockIdx.x;
  if (w < 64) role_F1(p, smem, rs, tag, w);
  else if (w < 128) role_F2(p, smem, rs, tag, w - 64);
  else role_A(p, smem, rs, tag, w - 128);
}

__global__ void print_kernel(unsigned char* ws) {
  const u64* dbg = (const u64*)(ws + WS_DBG);
  u64 t0 = ~0ull;
  for (int w = 0; w < 256; ++w) t0 = dbg[(w * 16) * 16] < t0 ? dbg[(w * 16) * 16] : t0;
  const int lo[3] = {0, 64, 128}, hi[3] = {64, 128, 256};
  for (int f = 0; f < 3; ++f)
    for (int i = 0; i < 16; ++i) {
      u64 mn = ~0ull, mx = 0, sum = 0; int n = 0;
      for (int w = lo[f]; w < hi[f]; ++w)
        for (int v = 0; v < 16; ++v) {
          const u64 x = dbg[(w * 16 + v) * 16 + i];
          if (x < t0 || x > t0 + 100000) continue;
          const u64 d = x - t0; mn = d < mn ? d : mn; mx = d > mx ? d : mx; sum += d; ++n;
        }
      if (n) printf("fam %d stamp %d: min %d avg %d max %d (x10ns)\n", f, i, (int)mn, (int)(sum / n), (int)mx);
    }
}

}

extern "C" void kernel_launch(void* const* d_in, const int* in_sizes, int n_in,
                              void* d_out, int out_size, void* d_ws, size_t ws_size,
                              hipStream_t stream) {
  Params p;
  p.x = (const u16*)d_in[0];
  p.K = (const u16*)d_in[1];
  p.V = (const u16*)d_in[2];
  p.wqA = (const u16*)d_in[3];
  p.woA = (const u16*)d_in[4];
  p.ckv = (const u16*)d_in[6];
  p.wqB = (const u16*)d_in[7];
  p.wuk = (const u16*)d_in[8];
  p.wuv = (const u16*)d_in[9];
  p.woB = (const u16*)d_in[10];
  p.gw = (const u16*)d_in[11];
  p.out = (u16*)d_out;
  p.ws = (unsigned char*)d_ws;
  hipLaunchKernelGGL(fused_kernel, dim3(256), dim3(1024), 0, stream, p);
  hipLaunchKernelGGL(print_kernel, dim3(1), dim3(1), 0, stream, p.ws);
}
```

```cpp
#include <hip/hip_runtime.h>
#include <hip/hip_bf16.h>
#include <stdint.h>

namespace {

typedef unsigned short u16;
typedef unsigned int u32;
typedef unsigned long long u64;
typedef u32 u32x4 __attribute__((ext_vector_type(4)));
typedef u32 u32x2 __attribute__((ext_vector_type(2)));
typedef float f32x4 __attribute__((ext_vector_type(4)));
typedef float f32x2 __attribute__((ext_vector_type(2)));
typedef float f32x16 __attribute__((ext_vector_type(16)));
typedef short s16x4 __attribute__((ext_vector_type(4)));
typedef __bf16 bf16x2 __attribute__((ext_vector_type(2)));
typedef __bf16 bf16x8 __attribute__((ext_vector_type(8)));
#define GAS __attribute__((address_space(1)))
#define LAS __attribute__((address_space(3)))
#define RLX_AGENT __ATOMIC_RELAXED, __HIP_MEMORY_SCOPE_AGENT
#define DI __device__ __forceinline__

constexpr u32 WS_EPOCH = 0;
constexpr u32 WS_F1FLAG = 256;
constexpr u32 WS_F2FLAG2 = 2048;
constexpr u32 WS_AFLAG = 3072;
constexpr u32 WS_F1FLAGC = 4096;
constexpr u32 WS_QA = 8192;
constexpr u32 WS_QABS = 8192 + 65536;
constexpr u32 WS_ML = WS_QABS + 131072;
constexpr u32 WS_OLAT = WS_ML + 16384;
constexpr u32 WS_AG = WS_OLAT + 2097152;
constexpr u32 WS_OG = WS_AG + 64 * 8 * 66 * 8;
constexpr u32 WS_END = WS_OG + 2 * 64 * 576 * 16;

constexpr int LDS_BYTES = 163840;
constexpr u32 QOFF = 131072;
constexpr float C2 = 0.08838834764831845f * 1.4426950408889634f;
constexpr u32 SPIN_CAP = 1u << 18;
#define A_SLOW 3
#define A_SLOWD 1
#define DMA_EARLY 4
#define A_DEPTH 4
#define WAITV_(n) asm volatile("s_waitcnt vmcnt(" #n ")" ::: "memory")
#define WAITV(n) WAITV_(n)
#define AD1_2 1
#define AD1_3 2
#define AD1_4 3
#define AD1_5 4
#define AD1_6 5
#define AD1__(d) AD1_##d
#define AD1_(d) AD1__(d)

#define BAR() asm volatile("s_waitcnt lgkmcnt(0)\n\ts_barrier" ::: "memory")
#define DRAIN() asm volatile("s_waitcnt vmcnt(0)" ::: "memory")
#define SCHED() __builtin_amdgcn_sched_barrier(0)

struct Params {
  const u16 *x, *K, *V, *wqA, *woA, *ckv, *wqB, *wuk, *wuv, *woB, *gw;
  u16* out;
  unsigned char* ws;
};

DI float bflo(u32 v) { return __uint_as_float(v << 16); }
DI float bfhi(u32 v) { return __uint_as_float(v & 0xffff0000u); }
DI u32 pk2(float lo, float hi) {
  bf16x2 r; r[0] = (__bf16)lo; r[1] = (__bf16)hi; return __builtin_bit_cast(u32, r);
}
DI u16 bf1(float v) { const __bf16 r = (__bf16)v; return __builtin_bit_cast(u16, r); }
DI float dot2(u32 a, u32 b, float acc) {
  return __builtin_amdgcn_fdot2_f32_bf16(__builtin_bit_cast(bf16x2, a), __builtin_bit_cast(bf16x2, b), acc, false);
}
DI float dot8(u32x4 a, u32x4 b) {
  float d = dot2(a[0], b[0], 0.f); d = dot2(a[1], b[1], d); d = dot2(a[2], b[2], d); return dot2(a[3], b[3], d);
}
template <int CTRL> DI float dpp(float x) {
  return __builtin_bit_cast(float, __builtin_amdgcn_mov_dpp(__builtin_bit_cast(int, x), CTRL, 0xf, 0xf, true));
}
constexpr int XOR1 = 0xB1, XOR2 = 0x4E, XOR7 = 0x141, ROR8 = 0x128, ROR4 = 0x124;
DI float tr32(float a, float b) {
  auto s = __builtin_amdgcn_permlane32_swap(__float_as_uint(a), __float_as_uint(b), false, false);
  return __uint_as_float(s[0]) + __uint_as_float(s[1]);
}
DI float tr16(float a, float b) {
  auto s = __builtin_amdgcn_permlane16_swap(__float_as_uint(a), __float_as_uint(b), false, false);
  return __uint_as_float(s[0]) + __uint_as_float(s[1]);
}
DI float x16_max(float x) {
  auto s = __builtin_amdgcn_permlane16_swap(__float_as_uint(x), __float_as_uint(x), false, false);
  return fmaxf(__uint_as_float(s[0]), __uint_as_float(s[1]));
}
DI float x32_max(float x) {
  auto s = __builtin_amdgcn_permlane32_swap(__float_as_uint(x), __float_as_uint(x), false, false);
  return fmaxf(__uint_as_float(s[0]), __uint_as_float(s[1]));
}
DI float x16_sum(float x) { return tr16(x, x); }
DI float x32_sum(float x) { return tr32(x, x); }
DI float ex2(float x) { return __builtin_amdgcn_exp2f(x); }

DI u32x4 ldg16(const u16* p) { return *(const GAS u32x4*)p; }
DI u32x4 ldg16nt(const u16* p) { return __builtin_nontemporal_load((const GAS u32x4*)p); }
DI u32x4 ld_sc1(__amdgpu_buffer_rsrc_t rs, u32 off) { return __builtin_amdgcn_raw_buffer_load_b128(rs, off, 0, 16); }
DI void st_sc1(__amdgpu_buffer_rsrc_t rs, u32 off, u32x4 v) { __builtin_amdgcn_raw_buffer_store_b128(v, rs, off, 0, 16); }
DI u64 ld8_sc1(unsigned char* ws, u32 off) { return __hip_atomic_load((GAS u64*)(ws + off), RLX_AGENT); }
DI void st8_sc1(unsigned char* ws, u32 off, u64 v) { __hip_atomic_store((GAS u64*)(ws + off), v, RLX_AGENT); }

DI u64 mix64(u64 e) {
  u64 z = e + 0x9E3779B97F4A7C15ull;
  z = (z ^ (z >> 30)) * 0xBF58476D1CE4E5B9ull;
  z = (z ^ (z >> 27)) * 0x94D049BB133111EBull;
  return z ^ (z >> 31);
}
DI void poll_flags(unsigned char* ws, u32 off, bool active, u64 tag) {
  for (u32 spins = 0; spins < SPIN_CAP; ++spins) {
    bool ok = true;
    if (active) ok = (ld8_sc1(ws, off) == tag);
    if (__all(ok)) break;
    __builtin_amdgcn_s_sleep(1);
  }
  __builtin_amdgcn_fence(__ATOMIC_ACQUIRE, "wavefront");
}
DI f32x2 spin_pair(__amdgpu_buffer_rsrc_t rs, u32 off, u32 tag32) {
  u32x4 v;
  for (u32 spins = 0; spins < SPIN_CAP; ++spins) {
    asm volatile("" ::: "memory");
    v = ld_sc1(rs, off);
    bool ok = (v[1] == tag32) & (v[3] == tag32);
    if (__all(ok)) break;
  }
  f32x2 r; r[0] = __uint_as_float(v[0]); r[1] = __uint_as_float(v[2]); return r;
}
DI void glds16(const void* gsrc, u32 lds_dst) {
  u32 keep;
  asm volatile("s_mov_b32 %0, m0\n\ts_mov_b32 m0, %2\n\ts_nop 0\n\tglobal_load_lds_dwordx4 %1, off nt\n\ts_mov_b32 m0, %0"
               : "=&s"(keep) : "v"(gsrc), "s"(lds_dst) : "memory");
}
DI u32 off_b(u32 row, u32 ch) { return 256u * row + 16u * (ch ^ (((row & 3) << 2) | ((row >> 2) & 3))); }
DI u32 wsw(u32 row) { return (((row >> 1) & 1) | (((row >> 3) & 1) << 1)) << 1; }
DI f32x4 mfma16(u32x4 a, u32x4 b, f32x4 c) {
  return __builtin_amdgcn_mfma_f32_16x16x32_bf16(__builtin_bit_cast(bf16x8, a), __builtin_bit_cast(bf16x8, b), c, 0, 0, 0);
}
DI f32x16 mfma32(u32x4 a, u32x4 b, f32x16 c) {
  return __builtin_amdgcn_mfma_f32_32x32x16_bf16(__builtin_bit_cast(bf16x8, a), __builtin_bit_cast(bf16x8, b), c, 0, 0, 0);
}
DI u32x4 tr_pair(const unsigned char* smem, u32 a0, u32 a1) {
  const s16x4 lo = __builtin_amdgcn_ds_read_tr16_b64_v4i16((LAS s16x4*)(smem + a0));
  const s16x4 hi = __builtin_amdgcn_ds_read_tr16_b64_v4i16((LAS s16x4*)(smem + a1));
  return __builtin_bit_cast(u32x4, __builtin_shufflevector(lo, hi, 0, 1, 2, 3, 4, 5, 6, 7));
}
DI u32x4 wfrag(const unsigned char* smem, u32 img, u32 stride, u32 ks, u32 ct, int lane) {
  const u32 g = lane >> 4, q = (lane & 15) >> 2, pp = lane & 3;
  const u32 r0 = 32 * ks + 8 * g + q, r1 = r0 + 4;
  const u32 ch = 2 * ct + (pp >> 1);
  return tr_pair(smem, img + r0 * stride + ((ch ^ wsw(r0)) << 4) + 8 * (pp & 1),
                       img + r1 * stride + ((ch ^ wsw(r1)) << 4) + 8 * (pp & 1));
}

#define STAMP(i) do {} while (0)

struct Gemv1 { u32x4 w[8]; u32x4 x0, x1; };
DI void gemv1_issue(Gemv1& g, const u16* W, const u16* x, int t) {
  const int rg = t >> 3, ch = t & 7;
  const u16* wp = W + (size_t)(8 * rg) * 4096 + 8 * ch;
  g.x0 = ldg16(x + 8 * rg);
  g.x1 = ldg16(x + 576 + 8 * rg);
#pragma unroll
  for (int k = 0; k < 8; ++k) g.w[k] = ldg16nt(wp + (size_t)k * 4096);
}
DI void gemv1_finish(Gemv1& g, float* red  , int wave, int lane) {
  float a0[8], a1[8];
#pragma unroll
  for (int e = 0; e < 8; ++e) { a0[e] = 0.f; a1[e] = 0.f; }
#pragma unroll
  for (int k = 0; k < 8; ++k) {
    const float xa = (k & 1) ? bfhi(g.x0[k >> 1]) : bflo(g.x0[k >> 1]);
    const float xb = (k & 1) ? bfhi(g.x1[k >> 1]) : bflo(g.x1[k >> 1]);
#pragma unroll
    for (int e = 0; e < 8; ++e) {
      const float wf = (e & 1) ? bfhi(g.w[k][e >> 1]) : bflo(g.w[k][e >> 1]);
      a0[e] = fmaf(xa, wf, a0[e]); a1[e] = fmaf(xb, wf, a1[e]);
    }
  }
  float v[8], u[4], wv[2];
#pragma unroll
  for (int e = 0; e < 8; ++e) v[e] = tr32(a0[e], a1[e]);
#pragma unroll
  for (int e = 0; e < 4; ++e) u[e] = tr16(v[e], v[e + 4]);
#pragma unroll
  for (int e = 0; e < 2; ++e) {
    const float t1 = u[e] + dpp<ROR8>(u[e]), t2 = u[e + 2] + dpp<ROR8>(u[e + 2]);
    wv[e] = (lane & 8) ? t2 : t1;
  }
  *(f32x2*)(red + wave * 128 + (lane >> 5) * 64 + (lane & 7) * 8 + ((lane >> 3) & 1) * 2 + ((lane >> 4) & 1) * 4) = f32x2{wv[0], wv[1]};
}
DI u32x4 gemv1_sum(const float* red, int t) {
  f32x4 s0 = {0.f, 0.f, 0.f, 0.f}, s1 = {0.f, 0.f, 0.f, 0.f};
  const float* r = red + (t >> 3) * 64 + (t & 7) * 8;
#pragma unroll
  for (int v = 0; v < 9; ++v) { s0 += *(const f32x4*)(r + v * 128); s1 += *(const f32x4*)(r + v * 128 + 4); }
  return u32x4{pk2(s0[0], s0[1]), pk2(s0[2], s0[3]), pk2(s1[0], s1[1]), pk2(s1[2], s1[3])};
}
DI void wo_dma(const u16* W, u32 lds_img, int wv, int lane) {
#pragma unroll 3
  for (int it = 0; it < 9; ++it) {
    const int n = wv * 9 + it;
    const u32 P = n * 1024 + lane * 16, row = P / 1152, cpos = (P - row * 1152) >> 4;
    glds16(W + (size_t)row * 576 + ((cpos ^ wsw(row)) << 3), lds_img + n * 1024);
  }
}
DI void oproj_mfma(const unsigned char* smem, u32 img, u32 zimg, __amdgpu_buffer_rsrc_t rs, u32 slot, u32 tag32, int wave, int lane) {
  u32x4 a[2];
#pragma unroll
  for (int ks = 0; ks < 2; ++ks) {
    a[ks] = u32x4{0u, 0u, 0u, 0u};
    if ((lane & 15) < 4) a[ks] = *(const u32x4*)(smem + zimg + (lane & 15) * 128 + (32 * ks + 8 * (lane >> 4)) * 2);
  }
  const int nt = wave < 4 ? 3 : 2;
  u32x4 bfr[3][2];
#pragma unroll
  for (int i = 0; i < 3; ++i)
    if (i < nt) {
#pragma unroll
      for (int ks = 0; ks < 2; ++ks) bfr[i][ks] = wfrag(smem, img, 1152, ks, wave + 16 * i, lane);
    }
#pragma unroll
  for (int i = 0; i < 3; ++i)
    if (i < nt) {
      f32x4 acc = {0.f, 0.f, 0.f, 0.f};
      acc = mfma16(a[0], bfr[i][0], acc);
      acc = mfma16(a[1], bfr[i][1], acc);
      if (lane < 16)
        st_sc1(rs, slot + (u32)(((wave + 16 * i) * 16 + lane) * 16),
               u32x4{__float_as_uint(acc[0] + acc[2]), tag32, __float_as_uint(acc[1] + acc[3]), tag32});
    }
}
DI void z_store(unsigned char* smem, u32 zimg, int b, int d, float z) {
  const u16 hi = bf1(z);
  const u16 lo = bf1(z - __uint_as_float((u32)hi << 16));
  *(u16*)(smem + zimg + b * 128 + d * 2) = hi;
  *(u16*)(smem + zimg + (2 + b) * 128 + d * 2) = lo;
}

#define TR_STEP(HALF, CTRL, BIT)                                                          \
  _Pragma("unroll") for (int h_ = 0; h_ < (HALF); ++h_) {                                 \
    const float lo_ = x[h_], hi_ = x[h_ + (HALF)];                                        \
    const float t1_ = lo_ + dpp<CTRL>(lo_), t2_ = hi_ + dpp<CTRL>(hi_);                   \
    x[h_] = (c & (BIT)) ? t2_ : t1_;                                                      \
  }

DI void role_A(const Params& p, unsigned char* smem, __amdgpu_buffer_rsrc_t rs, u64 tag, int a) {
  const int t = threadIdx.x, lane = t & 63, wave = __builtin_amdgcn_readfirstlane(t >> 6);
  const int r = lane >> 4, c = lane & 15;
  const int hkv = a & 15, b = (a >> 4) & 1, s = a >> 5;
  const size_t base = ((size_t)(b * 16 + hkv) * 4096 + s * 1024 + wave * 64 + r) * 128 + c * 8;
  const u16* Kp = p.K + base;
  const u16* Vp = p.V + base;
  unsigned char* qs = smem;
  float* pw = (float*)(smem + 1024) + wave * 128;
  float* mrg = (float*)(smem + 1024 + 8192);
  float* mls = mrg + 16 * 256;
  u32x4 kv[16];
  if (wave == 0) {
    const u32 ntag = ~(u32)tag;
#pragma unroll
    for (int k = 0; k < 3; ++k) {
      const int n = lane + 64 * k;
      if (n < 132) {
        const int seg = n / 33, pos = n - seg * 33;
        st_sc1(rs, WS_AG + (u32)((((hkv * 4 + seg) * 8 + b * 4 + s) * 66 + 2 * pos) * 8), u32x4{0u, ntag, 0u, ntag});
      }
    }
    DRAIN();
    if (lane == 0) st8_sc1(p.ws, WS_AFLAG + a * 8, tag);
    {
      const u32 thi = (u32)(tag >> 32), tlo = (u32)tag & 0xffff0000u;
      const u32 off = WS_QA + (u32)((b * 4096 + hkv * 256 + lane * 4) * 8);
      u32x4 g0, g1;
      for (u32 spins = 0; spins < SPIN_CAP; ++spins) {
        asm volatile("" ::: "memory");
        g0 = ld_sc1(rs, off); g1 = ld_sc1(rs, off + 16);
        const bool ok = (g0[1] == thi) & (g0[3] == thi) & (g1[1] == thi) & (g1[3] == thi) &
                        ((g0[0] & 0xffff0000u) == tlo) & ((g0[2] & 0xffff0000u) == tlo) &
                        ((g1[0] & 0xffff0000u) == tlo) & ((g1[2] & 0xffff0000u) == tlo);
        if (__all(ok)) break;
        __builtin_amdgcn_s_sleep(1);
      }
      *(u32x2*)(qs + lane * 8) = u32x2{(g0[0] & 0xffffu) | (g0[2] << 16), (g1[0] & 0xffffu) | (g1[2] << 16)};
    }
    STAMP(1);
    BAR();
#pragma unroll
    for (int i = 0; i < 16; ++i) kv[i] = ldg16nt(Kp + i * 512);
  } else {
#pragma unroll
    for (int i = 0; i < 16; ++i) {
      kv[i] = ldg16nt(Kp + i * 512);
      if (i < A_SLOW) {
        if (A_SLOWD == 1) asm volatile("s_waitcnt vmcnt(0)" ::: "memory");
        else if (i >= 1) asm volatile("s_waitcnt vmcnt(1)" ::: "memory");
      } else if (i >= A_DEPTH - 1 && i < 15) WAITV(AD1_(A_DEPTH));
    }
    STAMP(1);
    BAR();
  }
  STAMP(2);
  const u32x4 q0 = *(const u32x4*)(qs + c * 16), q1 = *(const u32x4*)(qs + 256 + c * 16);
  float sc[2];
#pragma unroll
  for (int G = 0; G < 2; ++G) {
    float x[16];
#pragma unroll
    for (int ii = 0; ii < 8; ++ii) {
      const int i = 8 * G + ii;
      x[2 * ii] = dot8(kv[i], q0);
      x[2 * ii + 1] = dot8(kv[i], q1);
      SCHED();
      kv[i] = ldg16nt(Vp + i * 512);
      SCHED();
    }
    TR_STEP(8, ROR8, 8) TR_STEP(4, XOR7, 4) TR_STEP(2, XOR2, 2) TR_STEP(1, XOR1, 1)
    sc[G] = x[0] * C2;
  }
  float m = fmaxf(sc[0], sc[1]);
  m = fmaxf(m, dpp<XOR2>(m)); m = fmaxf(m, dpp<ROR4>(m)); m = fmaxf(m, dpp<ROR8>(m));
  m = x32_max(x16_max(m));
  const float p0 = ex2(sc[0] - m), p1 = ex2(sc[1] - m);
  float l = p0 + p1;
  l += dpp<XOR2>(l); l += dpp<ROR4>(l); l += dpp<ROR8>(l);
  l = x32_sum(x16_sum(l));
  pw[r * 32 + c] = p0;
  pw[r * 32 + 16 + c] = p1;
  float a0[8], a1[8];
#pragma unroll
  for (int e = 0; e < 8; ++e) { a0[e] = 0.f; a1[e] = 0.f; }
#pragma unroll
  for (int i2 = 0; i2 < 8; ++i2) {
    const f32x4 pp = *(const f32x4*)(pw + r * 32 + i2 * 4);
#pragma unroll
    for (int ii = 0; ii < 2; ++ii) {
      const int i = 2 * i2 + ii;
      const float pa = pp[2 * ii], pb = pp[2 * ii + 1];
#pragma unroll
      for (int e = 0; e < 8; ++e) {
        const float vf = (e & 1) ? bfhi(kv[i][e >> 1]) : bflo(kv[i][e >> 1]);
        a0[e] = fmaf(pa, vf, a0[e]); a1[e] = fmaf(pb, vf, a1[e]);
      }
    }
  }
  STAMP(3);
  {
    float v[8];
#pragma unroll
    for (int e = 0; e < 8; ++e) v[e] = tr32(a0[e], a1[e]);
    f32x4 u;
#pragma unroll
    for (int e = 0; e < 4; ++e) u[e] = tr16(v[e], v[e + 4]);
    *(f32x4*)(mrg + wave * 256 + (lane >> 5) * 128 + c * 8 + ((lane >> 4) & 1) * 4) = u;
    if (lane < 2) *(f32x2*)(mls + wave * 4 + lane * 2) = f32x2{m, l};
  }
  BAR();
  if (t < 256) {
    const int g = t >> 7, d = t & 127;
    float mv[16], M = -INFINITY;
#pragma unroll
    for (int v = 0; v < 16; ++v) { mv[v] = mls[v * 4 + g * 2]; M = fmaxf(M, mv[v]); }
    float L = 0.f, A = 0.f;
#pragma unroll
    for (int v = 0; v < 16; ++v) {
      const float w = ex2(mv[v] - M);
      L = fmaf(w, mls[v * 4 + g * 2 + 1], L);
      A = fmaf(w, mrg[v * 256 + t], A);
    }
    const u32 tag32 = (u32)tag;
    const u32 slot = WS_AG + (u32)((((hkv * 2 + g) * 2 + (d >> 6)) * 8 + b * 4 + s) * 66 * 8);
    st8_sc1(p.ws, slot + (d & 63) * 8, ((u64)tag32 << 32) | __float_as_uint(A));
    if ((d & 63) < 2) st8_sc1(p.ws, slot + (64 + (d & 1)) * 8, ((u64)tag32 << 32) | __float_as_uint((d & 1) ? L : M));
  }
  STAMP(4);
}

DI void ckv_dma(const Params& p, u32 lds0, int b, int s, int w4, int lane, int it) {
  const int n = it * 4 + w4;
  const u32 row = 4 * (n >> 2) + (lane >> 4), cb = n & 3;
  const u32 ch = (lane & 15) ^ (((row & 3) << 2) | ((row >> 2) & 3));
  glds16(p.ckv + ((size_t)(b * 4096 + s * 128 + row) * 512 + cb * 128 + ch * 8), lds0 + cb * 32768 + (n >> 2) * 1024);
}
DI void role_F2(const Params& p, unsigned char* smem, __amdgpu_buffer_rsrc_t rs, u64 tag, int j) {
  const int t = threadIdx.x, lane = t & 63, wave = __builtin_amdgcn_readfirstlane(t >> 6);
  const int b = j & 1, s = j >> 1;
  float* red = (float*)(smem + QOFF);
  const u32 lds0 = (u32)(size_t)smem;
  const u32 tag32 = (u32)tag;
  const u32 oslot = WS_OG + (u32)(j * 9216);
  if (wave < 9) {
    Gemv1 g;
    gemv1_issue(g, p.wqA + 64 * j, p.x, t);
    gemv1_finish(g, red, wave, lane);
    STAMP(9);
  } else if (wave >= 12) {
    for (int it = 0; it < DMA_EARLY; ++it) {
      ckv_dma(p, lds0, b, s, wave - 12, lane, it);
      asm volatile("s_waitcnt vmcnt(1)" ::: "memory");
    }
  }
  STAMP(1);
  BAR();
  if (wave == 0) {
    f32x2 sq = {0.f, 0.f};
#pragma unroll
    for (int v = 0; v < 9; ++v) sq += *(const f32x2*)(red + v * 128 + 2 * lane);
    const u32 thi = (u32)(tag >> 32), tlo = (u32)tag & 0xffff0000u;
    st_sc1(rs, WS_QA + (u32)(((lane >> 5) * 4096 + 64 * j + (lane & 31) * 2) * 8),
           u32x4{tlo | bf1(sq[0]), thi, tlo | bf1(sq[1]), thi});
    STAMP(2);
  }
  if (wave < 9) {
    st_sc1(rs, oslot + t * 16, u32x4{0u, ~tag32, 0u, ~tag32});
  } else if (wave == 9) {
    poll_flags(p.ws, WS_AFLAG + ((j >> 2) + 16 * (lane & 7)) * 8, lane < 8, tag);
    poll_flags(p.ws, WS_F1FLAG + lane * 8, true, tag);
    STAMP(2);
  } else if (wave >= 12) {
#pragma unroll 4
    for (int it = DMA_EARLY; it < 32; ++it) { ckv_dma(p, lds0, b, s, wave - 12, lane, it); WAITV(7); }
  }
  BAR();
  {
    u32x4 v0[2], v1[2];
#pragma unroll
    for (int u = 0; u < 2; ++u) {
      const int n = t + 1024 * u;
      v0[u] = ld_sc1(rs, WS_QABS + (u32)((b * 2 + 0) * 32768 + n * 16));
      v1[u] = ld_sc1(rs, WS_QABS + (u32)((b * 2 + 1) * 32768 + n * 16));
    }
#pragma unroll
    for (int u = 0; u < 2; ++u) {
      const int n = t + 1024 * u;
      u32x4 o;
#pragma unroll
      for (int e = 0; e < 4; ++e) o[e] = pk2(bflo(v0[u][e]) + bflo(v1[u][e]), bfhi(v0[u][e]) + bfhi(v1[u][e]));
      const u32 h = n >> 6, c8 = n & 63;
      *(u32x4*)(smem + QOFF + (c8 >> 4) * 8192 + off_b(h, c8 & 15)) = o;
    }
    DRAIN();
  }
  STAMP(3);
  BAR();
  const int l31 = lane & 31, hf = lane >> 5;
  float* stat = (float*)(smem + QOFF + 8192);
  f32x16 sacc;
  float Mh = 0.f;
  if (wave < 4) {
#pragma unroll
    for (int i = 0; i < 16; ++i) sacc[i] = 0.f;
    u32x4 av[2][4], bv[2][4];
#define S_LOAD(BT)                                                                              \
  _Pragma("unroll") for (int u = 0; u < 4; ++u) {                                               \
    const u32 ks = 4 * (BT) + u, cb = ks >> 3, ss = ks & 7;                                      \
    av[(BT) & 1][u] = *(const u32x4*)(smem + cb * 32768 + off_b(32 * wave + l31, 2 * ss + hf));  \
    bv[(BT) & 1][u] = *(const u32x4*)(smem + QOFF + cb * 8192 + off_b(l31, 2 * ss + hf));        \
  }
    S_LOAD(0)
#pragma unroll
    for (int bt = 0; bt < 8; ++bt) {
      if (bt < 7) { S_LOAD(bt + 1) }
      SCHED();
#pragma unroll
      for (int u = 0; u < 4; ++u) sacc = mfma32(av[bt & 1][u], bv[bt & 1][u], sacc);
      SCHED();
    }
    float mx = -INFINITY;
#pragma unroll
    for (int i = 0; i < 16; ++i) { sacc[i] *= C2; mx = fmaxf(mx, sacc[i]); }
    Mh = x32_max(mx);
  }
  STAMP(10);
  BAR();
  if (wave < 4 && lane < 32) stat[wave * 32 + lane] = Mh;
  BAR();
  if (wave < 4) {
    Mh = fmaxf(fmaxf(stat[l31], stat[32 + l31]), fmaxf(stat[64 + l31], stat[96 + l31]));
    float ls = 0.f;
#pragma unroll
    for (int i = 0; i < 16; ++i) { sacc[i] = ex2(sacc[i] - Mh); ls += sacc[i]; }
    ls = x32_sum(ls);
    if (lane < 32) stat[128 + wave * 32 + lane] = ls;
#pragma unroll
    for (int g = 0; g < 4; ++g) {
      const u32x2 pv = {pk2(sacc[4 * g], sacc[4 * g + 1]), pk2(sacc[4 * g + 2], sacc[4 * g + 3])};
      *(u32x2*)(smem + QOFF + off_b(l31, 4 * wave + g) + 8 * hf) = pv;
    }
  }
  BAR();
  STAMP(11);
  const float Lh = (stat[128 + l31] + stat[160 + l31]) + (stat[192 + l31] + stat[224 + l31]);
  {
    f32x16 oacc;
#pragma unroll
    for (int i = 0; i < 16; ++i) oacc[i] = 0.f;
    const u32 cb = wave >> 2, ct = wave & 3;
    const u32 blk = (lane >> 4) & 1, q4 = (lane & 15) >> 2, p4 = lane & 3;
    u32x4 av[8], bv[8];
#pragma unroll
    for (int ks = 0; ks < 8; ++ks) {
      av[ks] = tr_pair(smem, cb * 32768 + off_b(16 * ks + 8 * hf + q4, 4 * ct + 2 * blk + (p4 >> 1)) + 8 * (p4 & 1),
                             cb * 32768 + off_b(16 * ks + 8 * hf + 4 + q4, 4 * ct + 2 * blk + (p4 >> 1)) + 8 * (p4 & 1));
      bv[ks] = *(const u32x4*)(smem + QOFF + off_b(l31, 2 * ks + hf));
    }
    SCHED();
#pragma unroll
    for (int ks = 0; ks < 8; ++ks) oacc = mfma32(av[ks], bv[ks], oacc);
    const float li = 1.f / Lh;
    STAMP(12);
    BAR();
#pragma unroll
    for (int g = 0; g < 4; ++g) {
      const u32x2 ov = {pk2(oacc[4 * g] * li, oacc[4 * g + 1] * li), pk2(oacc[4 * g + 2] * li, oacc[4 * g + 3] * li)};
      *(u32x2*)(smem + 81920 + l31 * 1040 + 64 * wave + 16 * g + 8 * hf) = ov;
    }
  }
  BAR();
  STAMP(13);
#pragma unroll
  for (int u = 0; u < 2; ++u) {
    const int h = 2 * wave + u;
    const u32x4 v = *(const u32x4*)(smem + 81920 + h * 1040 + lane * 16);
    st_sc1(rs, WS_OLAT + (u32)(((h * 2 + b) * 32 + s) * 1024 + lane * 16), v);
  }
  if (t < 32) st8_sc1(p.ws, WS_ML + (u32)(((t * 2 + b) * 32 + s) * 8), ((u64)__float_as_uint(Lh) << 32) | __float_as_uint(Mh));
  STAMP(14);
  DRAIN();
  STAMP(15);
  BAR();
  if (t == 0) st8_sc1(p.ws, WS_F2FLAG2 + j * 8, tag);
  STAMP(4);
  if (wave >= 8) { wo_dma(p.woA + (size_t)(64 * j) * 576, lds0, wave - 8, lane); DRAIN(); }
  float* ag = (float*)(smem + QOFF);
  constexpr u32 ZIMG = QOFF + 4096;
  if (t < 264) {
    const f32x2 v = spin_pair(rs, WS_AG + (u32)(j * 8 * 66 * 8 + t * 16), tag32);
    *(f32x2*)(ag + 2 * t) = v;
  }
  STAMP(5);
  BAR();
  if (t < 128) {
    const int bb = t >> 6, d = t & 63;
    const float* a = ag + bb * 4 * 66;
    const float m0 = a[64], m1 = a[66 + 64], m2 = a[132 + 64], m3 = a[198 + 64];
    const float M = fmaxf(fmaxf(m0, m1), fmaxf(m2, m3));
    const float w0 = ex2(m0 - M), w1 = ex2(m1 - M), w2 = ex2(m2 - M), w3 = ex2(m3 - M);
    const float L = w0 * a[65] + w1 * a[66 + 65] + w2 * a[132 + 65] + w3 * a[198 + 65];
    const float z = (w0 * a[d] + w1 * a[66 + d] + w2 * a[132 + d] + w3 * a[198 + d]) / L;
    z_store(smem, ZIMG, bb, d, z);
  }
  BAR();
  oproj_mfma(smem, 0, ZIMG, rs, oslot, tag32, wave, lane);
  STAMP(6);
}

DI void role_F1(const Params& p, unsigned char* smem, __amdgpu_buffer_rsrc_t rs, u64 tag, int w) {
  const int t = threadIdx.x, lane = t & 63, wave = __builtin_amdgcn_readfirstlane(t >> 6);
  const int h = w & 31, dh = w >> 5;
  const int col0 = h * 128 + dh * 64;
  constexpr u32 UV_OFF = 0, WO_OFF = 65536, AI_OFF = 139264, SM_OFF = 155648;
  constexpr u32 ZIMG = SM_OFF + 2048;
  float* red = (float*)(smem + AI_OFF);
  unsigned char* qsh = smem + AI_OFF + 5120;
  float* qas = (float*)(smem + AI_OFF + 8192);
  float* part = (float*)(smem + SM_OFF);
  float* gsh = (float*)(smem + SM_OFF + 2560);
  float* fin = (float*)(smem + SM_OFF + 3072);
  const u32 lds0 = (u32)(size_t)smem;
  const u32 tag32 = (u32)tag;
  const u32 oslot = WS_OG + (u32)((64 + w) * 9216);
  u32x4 uk[4];
  const int ch = t & 7;
  if (wave < 9) {
    Gemv1 g;
    gemv1_issue(g, p.wqB + col0, p.x, t);
#pragma unroll
    for (int k = 0; k < 4; ++k) uk[k] = ldg16nt(p.wuk + (size_t)((t >> 3) + 128 * k) * 4096 + col0 + 8 * ch);
    STAMP(8);
    gemv1_finish(g, red, wave, lane);
    STAMP(9);
  } else {
#pragma unroll
    for (int k = 0; k < 4; ++k) uk[k] = ldg16nt(p.wuk + (size_t)((t >> 3) + 128 * k) * 4096 + col0 + 8 * ch);
    if (wave >= 14) {
      const u16* xr = p.x + (wave - 14) * 576;
      float sg = 0.f;
#pragma unroll
      for (int k = 0; k < 9; ++k) {
        const int mm = lane + 64 * k;
        sg = fmaf(__uint_as_float((u32)xr[mm] << 16), __uint_as_float((u32)p.gw[mm] << 16), sg);
      }
      sg += dpp<XOR1>(sg); sg += dpp<XOR2>(sg); sg += dpp<ROR4>(sg); sg += dpp<ROR8>(sg);
      sg = x32_sum(x16_sum(sg));
      if (lane == 0) gsh[wave - 14] = 1.f / (1.f + __expf(-sg));
    }
  }
  BAR();
  if (t < 16) *(u32x4*)(qsh + t * 16) = gemv1_sum(red, t);
  BAR();
  {
    const u32x4 q0 = *(const u32x4*)(qsh + ch * 16), q1 = *(const u32x4*)(qsh + 128 + ch * 16);
    float x[8];
#pragma unroll
    for (int k = 0; k < 4; ++k) { x[2 * k] = dot8(uk[k], q0); x[2 * k + 1] = dot8(uk[k], q1); }
    const int c = ch;
    TR_STEP(4, XOR7, 4) TR_STEP(2, XOR2, 2) TR_STEP(1, XOR1, 1)
    qas[(ch & 1) * 512 + (t >> 3) + 128 * (ch >> 1)] = x[0];
  }
  STAMP(10);
  BAR();
  if (wave < 2) {
    const f32x4 v0 = *(const f32x4*)(qas + t * 8), v1 = *(const f32x4*)(qas + t * 8 + 4);
    const u32x4 o = {pk2(v0[0], v0[1]), pk2(v0[2], v0[3]), pk2(v1[0], v1[1]), pk2(v1[2], v1[3])};
    st_sc1(rs, WS_QABS + (u32)((((t >> 6) * 2 + dh) * 32 + h) * 1024 + (t & 63) * 16), o);
    DRAIN();
  }
  BAR();
  if (t == 0) st8_sc1(p.ws, WS_F1FLAG + w * 8, tag);
  STAMP(1);
  if (wave < 8) {
    wo_dma(p.woB + (size_t)col0 * 576, lds0 + WO_OFF, wave, lane);
  } else {
#pragma unroll
    for (int it = 0; it < 8; ++it) {
      const int n = (wave - 8) * 8 + it;
      const u32 row = n * 8 + (lane >> 3);
      glds16(p.wuv + (size_t)row * 4096 + col0 + (((lane & 7) ^ wsw(row)) << 3), lds0 + UV_OFF + n * 1024);
    }
  }
  if (t < 576) st_sc1(rs, oslot + t * 16, u32x4{0u, ~tag32, 0u, ~tag32});
  DRAIN();
  BAR();
  if (wave == 0) {
    if (lane == 0) st8_sc1(p.ws, WS_F1FLAGC + w * 8, tag);
    poll_flags(p.ws, WS_F1FLAGC + lane * 8, true, tag);
    poll_flags(p.ws, WS_F2FLAG2 + lane * 8, true, tag);
  }
  STAMP(2);
  BAR();
  {
    const int bb = wave >> 3, sg = wave & 7;
    u32x4 pv[4];
#pragma unroll
    for (int u = 0; u < 4; ++u) pv[u] = ld_sc1(rs, WS_OLAT + (u32)(((h * 2 + bb) * 32 + 4 * sg + u) * 1024 + lane * 16));
    const u64 ml = ld8_sc1(p.ws, WS_ML + (u32)(((h * 2 + bb) * 32 + (lane & 31)) * 8));
    const float ms = __uint_as_float((u32)ml), ls = __uint_as_float((u32)(ml >> 32));
    float M = ms;
    M = fmaxf(M, dpp<XOR1>(M)); M = fmaxf(M, dpp<XOR2>(M)); M = fmaxf(M, dpp<ROR4>(M)); M = fmaxf(M, dpp<ROR8>(M));
    M = x16_max(M);
    const float wl = ls * ex2(ms - M);
    float L = wl;
    L += dpp<XOR1>(L); L += dpp<XOR2>(L); L += dpp<ROR4>(L); L += dpp<ROR8>(L);
    L = x16_sum(L);
    const float wn = wl / L;
    float a[8];
#pragma unroll
    for (int e = 0; e < 8; ++e) a[e] = 0.f;
#pragma unroll
    for (int u = 0; u < 4; ++u) {
      const float wu = __shfl(wn, 4 * sg + u);
#pragma unroll
      for (int e = 0; e < 8; ++e) a[e] = fmaf(wu, (e & 1) ? bfhi(pv[u][e >> 1]) : bflo(pv[u][e >> 1]), a[e]);
    }
    *(u32x4*)(smem + AI_OFF + (lane >> 4) * 4096 + off_b(wave, lane & 15)) =
        u32x4{pk2(a[0], a[1]), pk2(a[2], a[3]), pk2(a[4], a[5]), pk2(a[6], a[7])};
  }
  STAMP(3);
  BAR();
  {
    const int nt = wave & 3, kq = wave >> 2;
    u32x4 af[4], bf[4];
#pragma unroll
    for (int u = 0; u < 4; ++u) {
      af[u] = *(const u32x4*)(smem + AI_OFF + kq * 4096 + off_b(lane & 15, 4 * u + (lane >> 4)));
      bf[u] = wfrag(smem, UV_OFF, 128, 4 * kq + u, nt, lane);
    }
    f32x4 acc = {0.f, 0.f, 0.f, 0.f};
#pragma unroll
    for (int u = 0; u < 4; ++u) acc = mfma16(af[u], bf[u], acc);
    const float sv = x16_sum((acc[0] + acc[1]) + (acc[2] + acc[3]));
    if ((lane & 16) == 0) part[(kq * 2 + (lane >> 5)) * 64 + nt * 16 + (lane & 15)] = sv;
  }
  STAMP(12);
  BAR();
  if (t < 128) z_store(smem, ZIMG, t >> 6, t & 63, (part[t] + part[128 + t]) + (part[256 + t] + part[384 + t]));
  BAR();
  STAMP(13);
  oproj_mfma(smem, WO_OFF, ZIMG, rs, oslot, tag32, wave, lane);
  STAMP(4);
  if (t < 576) {
    const int pp = t / 9, i = t - pp * 9;
    const u32 off = WS_OG + (u32)(pp * 9216 + (9 * w + i) * 16);
    const f32x2 vb = spin_pair(rs, off + 64 * 9216, tag32);
    STAMP(5);
    const f32x2 va = spin_pair(rs, off, tag32);
    const float g0 = gsh[0], g1 = gsh[1];
    *(f32x2*)(fin + 2 * t) = f32x2{g0 * va[0] + (1.f - g0) * vb[0], g1 * va[1] + (1.f - g1) * vb[1]};
  }
  STAMP(6);
  BAR();
  if (t < 192) {
    const int o = t >> 3, k = t & 7;
    float sum = 0.f;
    if (o < 18) {
#pragma unroll
      for (int u = 0; u < 8; ++u) sum += fin[(k * 8 + u) * 18 + o];
    }
    sum += dpp<XOR1>(sum); sum += dpp<XOR2>(sum); sum += dpp<XOR7>(sum);
    if (k == 0 && o < 18) p.out[(o & 1) * 576 + 9 * w + (o >> 1)] = bf1(sum);
    if (w == 0 && t == 0) st8_sc1(p.ws, WS_EPOCH, tag);
  }
  STAMP(7);
}

__global__ void __launch_bounds__(1024, 4) fused_kernel(Params p) {
  __shared__ __attribute__((aligned(16))) unsigned char smem[LDS_BYTES];
  const u64 epoch = *(const __attribute__((address_space(4))) u64*)(p.ws + WS_EPOCH);
  STAMP(0);
  const u64 tag = mix64(epoch);
  const __amdgpu_buffer_rsrc_t rs = __builtin_amdgcn_make_buffer_rsrc(p.ws, 0, WS_END, 0x00020000);
  const int w = blockIdx.x;
  if (w < 64) role_F1(p, smem, rs, tag, w);
  else if (w < 128) role_F2(p, smem, rs, tag, w - 64);
  else role_A(p, smem, rs, tag, w - 128);
}

}

extern "C" void kernel_launch(void* const* d_in, const int* in_sizes, int n_in,
                              void* d_out, int out_size, void* d_ws, size_t ws_size,
                              hipStream_t stream) {
  Params p;
  p.x = (const u16*)d_in[0];
  p.K = (const u16*)d_in[1];
  p.V = (const u16*)d_in[2];
  p.wqA = (const u16*)d_in[3];
  p.woA = (const u16*)d_in[4];
  p.ckv = (const u16*)d_in[6];
  p.wqB = (const u16*)d_in[7];
  p.wuk = (const u16*)d_in[8];
  p.wuv = (const u16*)d_in[9];
  p.woB = (const u16*)d_in[10];
  p.gw = (const u16*)d_in[11];
  p.out = (u16*)d_out;
  p.ws = (unsigned char*)d_ws;
  hipLaunchKernelGGL(fused_kernel, dim3(256), dim3(1024), 0, stream, p);
}
```

```cpp
#define DBG_STAMPS
#include <hip/hip_runtime.h>
#include <hip/hip_bf16.h>
#include <stdint.h>

namespace {

typedef unsigned short u16;
typedef unsigned int u32;
typedef unsigned long long u64;
typedef u32 u32x4 __attribute__((ext_vector_type(4)));
typedef u32 u32x2 __attribute__((ext_vector_type(2)));
typedef float f32x4 __attribute__((ext_vector_type(4)));
typedef float f32x2 __attribute__((ext_vector_type(2)));
typedef float f32x16 __attribute__((ext_vector_type(16)));
typedef short s16x4 __attribute__((ext_vector_type(4)));
typedef __bf16 bf16x2 __attribute__((ext_vector_type(2)));
typedef __bf16 bf16x8 __attribute__((ext_vector_type(8)));
#define GAS __attribute__((address_space(1)))
#define LAS __attribute__((address_space(3)))
#define RLX_AGENT __ATOMIC_RELAXED, __HIP_MEMORY_SCOPE_AGENT
#define DI __device__ __forceinline__

constexpr u32 WS_EPOCH = 0;
constexpr u32 WS_F1FLAG = 256;
constexpr u32 WS_F2FLAG2 = 2048;
constexpr u32 WS_AFLAG = 3072;
constexpr u32 WS_F1FLAGC = 4096;
constexpr u32 WS_QA = 8192;
constexpr u32 WS_QABS = 8192 + 65536;
constexpr u32 WS_ML = WS_QABS + 131072;
constexpr u32 WS_OLAT = WS_ML + 16384;
constexpr u32 WS_AG = WS_OLAT + 2097152;
constexpr u32 WS_OG = WS_AG + 64 * 8 * 66 * 8;
constexpr u32 WS_END = WS_OG + 2 * 64 * 576 * 16;
constexpr u32 WS_DBG = 8u << 20;

constexpr int LDS_BYTES = 163840;
constexpr u32 QOFF = 131072;
constexpr float C2 = 0.08838834764831845f * 1.4426950408889634f;
constexpr u32 SPIN_CAP = 1u << 18;
#define A_SLOW 3
#define A_SLOWD 1
#define DMA_EARLY 4
#define A_DEPTH 4
#define WAITV_(n) asm volatile("s_waitcnt vmcnt(" #n ")" ::: "memory")
#define WAITV(n) WAITV_(n)
#define AD1_2 1
#define AD1_3 2
#define AD1_4 3
#define AD1_5 4
#define AD1_6 5
#define AD1__(d) AD1_##d
#define AD1_(d) AD1__(d)

#define BAR() asm volatile("s_waitcnt lgkmcnt(0)\n\ts_barrier" ::: "memory")
#define DRAIN() asm volatile("s_waitcnt vmcnt(0)" ::: "memory")
#define SCHED() __builtin_amdgcn_sched_barrier(0)

struct Params {
  const u16 *x, *K, *V, *wqA, *woA, *ckv, *wqB, *wuk, *wuv, *woB, *gw;
  u16* out;
  unsigned char* ws;
};

DI float bflo(u32 v) { return __uint_as_float(v << 16); }
DI float bfhi(u32 v) { return __uint_as_float(v & 0xffff0000u); }
DI u32 pk2(float lo, float hi) {
  bf16x2 r; r[0] = (__bf16)lo; r[1] = (__bf16)hi; return __builtin_bit_cast(u32, r);
}
DI u16 bf1(float v) { const __bf16 r = (__bf16)v; return __builtin_bit_cast(u16, r); }
DI float dot2(u32 a, u32 b, float acc) {
  return __builtin_amdgcn_fdot2_f32_bf16(__builtin_bit_cast(bf16x2, a), __builtin_bit_cast(bf16x2, b), acc, false);
}
DI float dot8(u32x4 a, u32x4 b) {
  float d = dot2(a[0], b[0], 0.f); d = dot2(a[1], b[1], d); d = dot2(a[2], b[2], d); return dot2(a[3], b[3], d);
}
template <int CTRL> DI float dpp(float x) {
  return __builtin_bit_cast(float, __builtin_amdgcn_mov_dpp(__builtin_bit_cast(int, x), CTRL, 0xf, 0xf, true));
}
constexpr int XOR1 = 0xB1, XOR2 = 0x4E, XOR7 = 0x141, ROR8 = 0x128, ROR4 = 0x124;
DI float tr32(float a, float b) {
  auto s = __builtin_amdgcn_permlane32_swap(__float_as_uint(a), __float_as_uint(b), false, false);
  return __uint_as_float(s[0]) + __uint_as_float(s[1]);
}
DI float tr16(float a, float b) {
  auto s = __builtin_amdgcn_permlane16_swap(__float_as_uint(a), __float_as_uint(b), false, false);
  return __uint_as_float(s[0]) + __uint_as_float(s[1]);
}
DI float x16_max(float x) {
  auto s = __builtin_amdgcn_permlane16_swap(__float_as_uint(x), __float_as_uint(x), false, false);
  return fmaxf(__uint_as_float(s[0]), __uint_as_float(s[1]));
}
DI float x32_max(float x) {
  auto s = __builtin_amdgcn_permlane32_swap(__float_as_uint(x), __float_as_uint(x), false, false);
  return fmaxf(__uint_as_float(s[0]), __uint_as_float(s[1]));
}
DI float x16_sum(float x) { return tr16(x, x); }
DI float x32_sum(float x) { return tr32(x, x); }
DI float ex2(float x) { return __builtin_amdgcn_exp2f(x); }

DI u32x4 ldg16(const u16* p) { return *(const GAS u32x4*)p; }
DI u32x4 ldg16nt(const u16* p) { return __builtin_nontemporal_load((const GAS u32x4*)p); }
DI u32x4 ld_sc1(__amdgpu_buffer_rsrc_t rs, u32 off) { return __builtin_amdgcn_raw_buffer_load_b128(rs, off, 0, 16); }
DI void st_sc1(__amdgpu_buffer_rsrc_t rs, u32 off, u32x4 v) { __builtin_amdgcn_raw_buffer_store_b128(v, rs, off, 0, 16); }
DI u64 ld8_sc1(unsigned char* ws, u32 off) { return __hip_atomic_load((GAS u64*)(ws + off), RLX_AGENT); }
DI void st8_sc1(unsigned char* ws, u32 off, u64 v) { __hip_atomic_store((GAS u64*)(ws + off), v, RLX_AGENT); }

DI u64 mix64(u64 e) {
  u64 z = e + 0x9E3779B97F4A7C15ull;
  z = (z ^ (z >> 30)) * 0xBF58476D1CE4E5B9ull;
  z = (z ^ (z >> 27)) * 0x94D049BB133111EBull;
  return z ^ (z >> 31);
}
DI void poll_flags(unsigned char* ws, u32 off, bool active, u64 tag) {
  for (u32 spins = 0; spins < SPIN_CAP; ++spins) {
    bool ok = true;
    if (active) ok = (ld8_sc1(ws, off) == tag);
    if (__all(ok)) break;
    __builtin_amdgcn_s_sleep(1);
  }
  __builtin_amdgcn_fence(__ATOMIC_ACQUIRE, "wavefront");
}
DI f32x2 spin_pair(__amdgpu_buffer_rsrc_t rs, u32 off, u32 tag32) {
  u32x4 v;
  for (u32 spins = 0; spins < SPIN_CAP; ++spins) {
    asm volatile("" ::: "memory");
    v = ld_sc1(rs, off);
    bool ok = (v[1] == tag32) & (v[3] == tag32);
    if (__all(ok)) break;
  }
  f32x2 r; r[0] = __uint_as_float(v[0]); r[1] = __uint_as_float(v[2]); return r;
}
DI void glds16(const void* gsrc, u32 lds_dst) {
  u32 keep;
  asm volatile("s_mov_b32 %0, m0\n\ts_mov_b32 m0, %2\n\ts_nop 0\n\tglobal_load_lds_dwordx4 %1, off nt\n\ts_mov_b32 m0, %0"
               : "=&s"(keep) : "v"(gsrc), "s"(lds_dst) : "memory");
}
DI u32 off_b(u32 row, u32 ch) { return 256u * row + 16u * (ch ^ (((row & 3) << 2) | ((row >> 2) & 3))); }
DI u32 wsw(u32 row) { return (((row >> 1) & 1) | (((row >> 3) & 1) << 1)) << 1; }
DI f32x4 mfma16(u32x4 a, u32x4 b, f32x4 c) {
  return __builtin_amdgcn_mfma_f32_16x16x32_bf16(__builtin_bit_cast(bf16x8, a), __builtin_bit_cast(bf16x8, b), c, 0, 0, 0);
}
DI f32x16 mfma32(u32x4 a, u32x4 b, f32x16 c) {
  return __builtin_amdgcn_mfma_f32_32x32x16_bf16(__builtin_bit_cast(bf16x8, a), __builtin_bit_cast(bf16x8, b), c, 0, 0, 0);
}
DI u32x4 tr_pair(const unsigned char* smem, u32 a0, u32 a1) {
  const s16x4 lo = __builtin_amdgcn_ds_read_tr16_b64_v4i16((LAS s16x4*)(smem + a0));
  const s16x4 hi = __builtin_amdgcn_ds_read_tr16_b64_v4i16((LAS s16x4*)(smem + a1));
  return __builtin_bit_cast(u32x4, __builtin_shufflevector(lo, hi, 0, 1, 2, 3, 4, 5, 6, 7));
}
DI u32x4 wfrag(const unsigned char* smem, u32 img, u32 stride, u32 ks, u32 ct, int lane) {
  const u32 g = lane >> 4, q = (lane & 15) >> 2, pp = lane & 3;
  const u32 r0 = 32 * ks + 8 * g + q, r1 = r0 + 4;
  const u32 ch = 2 * ct + (pp >> 1);
  return tr_pair(smem, img + r0 * stride + ((ch ^ wsw(r0)) << 4) + 8 * (pp & 1),
                       img + r1 * stride + ((ch ^ wsw(r1)) << 4) + 8 * (pp & 1));
}

#define STAMP(i) do { if ((threadIdx.x & 63) == 0) ((GAS u64*)(p.ws + WS_DBG))[(blockIdx.x * 16 + (threadIdx.x >> 6)) * 16 + (i)] = __builtin_amdgcn_s_memrealtime(); } while (0)

struct Gemv1 { u32x4 w[8]; u32x4 x0, x1; };
DI void gemv1_issue(Gemv1& g, const u16* W, const u16* x, int t) {
  const int rg = t >> 3, ch = t & 7;
  const u16* wp = W + (size_t)(8 * rg) * 4096 + 8 * ch;
  g.x0 = ldg16(x + 8 * rg);
  g.x1 = ldg16(x + 576 + 8 * rg);
#pragma unroll
  for (int k = 0; k < 8; ++k) g.w[k] = ldg16nt(wp + (size_t)k * 4096);
}
DI void gemv1_finish(Gemv1& g, float* red  , int wave, int lane) {
  float a0[8], a1[8];
#pragma unroll
  for (int e = 0; e < 8; ++e) { a0[e] = 0.f; a1[e] = 0.f; }
#pragma unroll
  for (int k = 0; k < 8; ++k) {
    const float xa = (k & 1) ? bfhi(g.x0[k >> 1]) : bflo(g.x0[k >> 1]);
    const float xb = (k & 1) ? bfhi(g.x1[k >> 1]) : bflo(g.x1[k >> 1]);
#pragma unroll
    for (int e = 0; e < 8; ++e) {
      const float wf = (e & 1) ? bfhi(g.w[k][e >> 1]) : bflo(g.w[k][e >> 1]);
      a0[e] = fmaf(xa, wf, a0[e]); a1[e] = fmaf(xb, wf, a1[e]);
    }
  }
  float v[8], u[4], wv[2];
#pragma unroll
  for (int e = 0; e < 8; ++e) v[e] = tr32(a0[e], a1[e]);
#pragma unroll
  for (int e = 0; e < 4; ++e) u[e] = tr16(v[e], v[e + 4]);
#pragma unroll
  for (int e = 0; e < 2; ++e) {
    const float t1 = u[e] + dpp<ROR8>(u[e]), t2 = u[e + 2] + dpp<ROR8>(u[e + 2]);
    wv[e] = (lane & 8) ? t2 : t1;
  }
  *(f32x2*)(red + wave * 128 + (lane >> 5) * 64 + (lane & 7) * 8 + ((lane >> 3) & 1) * 2 + ((lane >> 4) & 1) * 4) = f32x2{wv[0], wv[1]};
}
DI u32x4 gemv1_sum(const float* red, int t) {
  f32x4 s0 = {0.f, 0.f, 0.f, 0.f}, s1 = {0.f, 0.f, 0.f, 0.f};
  const float* r = red + (t >> 3) * 64 + (t & 7) * 8;
#pragma unroll
  for (int v = 0; v < 9; ++v) { s0 += *(const f32x4*)(r + v * 128); s1 += *(const f32x4*)(r + v * 128 + 4); }
  return u32x4{pk2(s0[0], s0[1]), pk2(s0[2], s0[3]), pk2(s1[0], s1[1]), pk2(s1[2], s1[3])};
}
DI void wo_dma(const u16* W, u32 lds_img, int wv, int lane) {
#pragma unroll 3
  for (int it = 0; it < 9; ++it) {
    const int n = wv * 9 + it;
    const u32 P = n * 1024 + lane * 16, row = P / 1152, cpos = (P - row * 1152) >> 4;
    glds16(W + (size_t)row * 576 + ((cpos ^ wsw(row)) << 3), lds_img + n * 1024);
  }
}
DI void oproj_mfma(const unsigned char* smem, u32 img, u32 zimg, __amdgpu_buffer_rsrc_t rs, u32 slot, u32 tag32, int wave, int lane) {
  u32x4 a[2];
#pragma unroll
  for (int ks = 0; ks < 2; ++ks) {
    a[ks] = u32x4{0u, 0u, 0u, 0u};
    if ((lane & 15) < 4) a[ks] = *(const u32x4*)(smem + zimg + (lane & 15) * 128 + (32 * ks + 8 * (lane >> 4)) * 2);
  }
  const int nt = wave < 4 ? 3 : 2;
  u32x4 bfr[3][2];
#pragma unroll
  for (int i = 0; i < 3; ++i)
    if (i < nt) {
#pragma unroll
      for (int ks = 0; ks < 2; ++ks) bfr[i][ks] = wfrag(smem, img, 1152, ks, wave + 16 * i, lane);
    }
#pragma unroll
  for (int i = 0; i < 3; ++i)
    if (i < nt) {
      f32x4 acc = {0.f, 0.f, 0.f, 0.f};
      acc = mfma16(a[0], bfr[i][0], acc);
      acc = mfma16(a[1], bfr[i][1], acc);
      if (lane < 16)
        st_sc1(rs, slot + (u32)(((wave + 16 * i) * 16 + lane) * 16),
               u32x4{__float_as_uint(acc[0] + acc[2]), tag32, __float_as_uint(acc[1] + acc[3]), tag32});
    }
}
DI void z_store(unsigned char* smem, u32 zimg, int b, int d, float z) {
  const u16 hi = bf1(z);
  const u16 lo = bf1(z - __uint_as_float((u32)hi << 16));
  *(u16*)(smem + zimg + b * 128 + d * 2) = hi;
  *(u16*)(smem + zimg + (2 + b) * 128 + d * 2) = lo;
}

#define TR_STEP(HALF, CTRL, BIT)                                                          \
  _Pragma("unroll") for (int h_ = 0; h_ < (HALF); ++h_) {                                 \
    const float lo_ = x[h_], hi_ = x[h_ + (HALF)];                                        \
    const float t1_ = lo_ + dpp<CTRL>(lo_), t2_ = hi_ + dpp<CTRL>(hi_);                   \
    x[h_] = (c & (BIT)) ? t2_ : t1_;                                                      \
  }

DI void role_A(const Params& p, unsigned char* smem, __amdgpu_buffer_rsrc_t rs, u64 tag, int a) {
  const int t = threadIdx.x, lane = t & 63, wave = __builtin_amdgcn_readfirstlane(t >> 6);
  const int r = lane >> 4, c = lane & 15;
  const int hkv = a & 15, b = (a >> 4) & 1, s = a >> 5;
  const size_t base = ((size_t)(b * 16 + hkv) * 4096 + s * 1024 + wave * 64 + r) * 128 + c * 8;
  const u16* Kp = p.K + base;
  const u16* Vp = p.V + base;
  unsigned char* qs = smem;
  float* pw = (float*)(smem + 1024) + wave * 128;
  float* mrg = (float*)(smem + 1024 + 8192);
  float* mls = mrg + 16 * 256;
  u32x4 kv[16];
  if (wave == 0) {
    const u32 ntag = ~(u32)tag;
#pragma unroll
    for (int k = 0; k < 3; ++k) {
      const int n = lane + 64 * k;
      if (n < 132) {
        const int seg = n / 33, pos = n - seg * 33;
        st_sc1(rs, WS_AG + (u32)((((hkv * 4 + seg) * 8 + b * 4 + s) * 66 + 2 * pos) * 8), u32x4{0u, ntag, 0u, ntag});
      }
    }
    DRAIN();
    if (lane == 0) st8_sc1(p.ws, WS_AFLAG + a * 8, tag);
    {
      const u32 thi = (u32)(tag >> 32), tlo = (u32)tag & 0xffff0000u;
      const u32 off = WS_QA + (u32)((b * 4096 + hkv * 256 + lane * 4) * 8);
      u32x4 g0, g1;
      for (u32 spins = 0; spins < SPIN_CAP; ++spins) {
        asm volatile("" ::: "memory");
        g0 = ld_sc1(rs, off); g1 = ld_sc1(rs, off + 16);
        const bool ok = (g0[1] == thi) & (g0[3] == thi) & (g1[1] == thi) & (g1[3] == thi) &
                        ((g0[0] & 0xffff0000u) == tlo) & ((g0[2] & 0xffff0000u) == tlo) &
                        ((g1[0] & 0xffff0000u) == tlo) & ((g1[2] & 0xffff0000u) == tlo);
        if (__all(ok)) break;
        __builtin_amdgcn_s_sleep(1);
      }
      *(u32x2*)(qs + lane * 8) = u32x2{(g0[0] & 0xffffu) | (g0[2] << 16), (g1[0] & 0xffffu) | (g1[2] << 16)};
    }
    STAMP(1);
    BAR();
#pragma unroll
    for (int i = 0; i < 16; ++i) kv[i] = ldg16nt(Kp + i * 512);
  } else {
#pragma unroll
    for (int i = 0; i < 16; ++i) {
      kv[i] = ldg16nt(Kp + i * 512);
      if (i < A_SLOW) {
        if (A_SLOWD == 1) asm volatile("s_waitcnt vmcnt(0)" ::: "memory");
        else if (i >= 1) asm volatile("s_waitcnt vmcnt(1)" ::: "memory");
      } else if (i >= A_DEPTH - 1 && i < 15) WAITV(AD1_(A_DEPTH));
    }
    STAMP(1);
    BAR();
  }
  STAMP(2);
  const u32x4 q0 = *(const u32x4*)(qs + c * 16), q1 = *(const u32x4*)(qs + 256 + c * 16);
  float sc[2];
#pragma unroll
  for (int G = 0; G < 2; ++G) {
    float x[16];
#pragma unroll
    for (int ii = 0; ii < 8; ++ii) {
      const int i = 8 * G + ii;
      x[2 * ii] = dot8(kv[i], q0);
      x[2 * ii + 1] = dot8(kv[i], q1);
      SCHED();
      kv[i] = ldg16nt(Vp + i * 512);
      SCHED();
    }
    TR_STEP(8, ROR8, 8) TR_STEP(4, XOR7, 4) TR_STEP(2, XOR2, 2) TR_STEP(1, XOR1, 1)
    sc[G] = x[0] * C2;
  }
  float m = fmaxf(sc[0], sc[1]);
  m = fmaxf(m, dpp<XOR2>(m)); m = fmaxf(m, dpp<ROR4>(m)); m = fmaxf(m, dpp<ROR8>(m));
  m = x32_max(x16_max(m));
  const float p0 = ex2(sc[0] - m), p1 = ex2(sc[1] - m);
  float l = p0 + p1;
  l += dpp<XOR2>(l); l += dpp<ROR4>(l); l += dpp<ROR8>(l);
  l = x32_sum(x16_sum(l));
  pw[r * 32 + c] = p0;
  pw[r * 32 + 16 + c] = p1;
  float a0[8], a1[8];
#pragma unroll
  for (int e = 0; e < 8; ++e) { a0[e] = 0.f; a1[e] = 0.f; }
#pragma unroll
  for (int i2 = 0; i2 < 8; ++i2) {
    const f32x4 pp = *(const f32x4*)(pw + r * 32 + i2 * 4);
#pragma unroll
    for (int ii = 0; ii < 2; ++ii) {
      const int i = 2 * i2 + ii;
      const float pa = pp[2 * ii], pb = pp[2 * ii + 1];
#pragma unroll
      for (int e = 0; e < 8; ++e) {
        const float vf = (e & 1) ? bfhi(kv[i][e >> 1]) : bflo(kv[i][e >> 1]);
        a0[e] = fmaf(pa, vf, a0[e]); a1[e] = fmaf(pb, vf, a1[e]);
      }
    }
  }
  STAMP(3);
  {
    float v[8];
#pragma unroll
    for (int e = 0; e < 8; ++e) v[e] = tr32(a0[e], a1[e]);
    f32x4 u;
#pragma unroll
    for (int e = 0; e < 4; ++e) u[e] = tr16(v[e], v[e + 4]);
    *(f32x4*)(mrg + wave * 256 + (lane >> 5) * 128 + c * 8 + ((lane >> 4) & 1) * 4) = u;
    if (lane < 2) *(f32x2*)(mls + wave * 4 + lane * 2) = f32x2{m, l};
  }
  BAR();
  if (t < 256) {
    const int g = t >> 7, d = t & 127;
    float mv[16], M = -INFINITY;
#pragma unroll
    for (int v = 0; v < 16; ++v) { mv[v] = mls[v * 4 + g * 2]; M = fmaxf(M, mv[v]); }
    float L = 0.f, A = 0.f;
#pragma unroll
    for (int v = 0; v < 16; ++v) {
      const float w = ex2(mv[v] - M);
      L = fmaf(w, mls[v * 4 + g * 2 + 1], L);
      A = fmaf(w, mrg[v * 256 + t], A);
    }
    const u32 tag32 = (u32)tag;
    const u32 slot = WS_AG + (u32)((((hkv * 2 + g) * 2 + (d >> 6)) * 8 + b * 4 + s) * 66 * 8);
    st8_sc1(p.ws, slot + (d & 63) * 8, ((u64)tag32 << 32) | __float_as_uint(A));
    if ((d & 63) < 2) st8_sc1(p.ws, slot + (64 + (d & 1)) * 8, ((u64)tag32 << 32) | __float_as_uint((d & 1) ? L : M));
  }
  STAMP(4);
}

DI void ckv_dma(const Params& p, u32 lds0, int b, int s, int w4, int lane, int it) {
  const int n = it * 4 + w4;
  const u32 row = 4 * (n >> 2) + (lane >> 4), cb = n & 3;
  const u32 ch = (lane & 15) ^ (((row & 3) << 2) | ((row >> 2) & 3));
  glds16(p.ckv + ((size_t)(b * 4096 + s * 128 + row) * 512 + cb * 128 + ch * 8), lds0 + cb * 32768 + (n >> 2) * 1024);
}
DI void role_F2(const Params& p, unsigned char* smem, __amdgpu_buffer_rsrc_t rs, u64 tag, int j) {
  const int t = threadIdx.x, lane = t & 63, wave = __builtin_amdgcn_readfirstlane(t >> 6);
  const int b = j & 1, s = j >> 1;
  float* red = (float*)(smem + QOFF);
  const u32 lds0 = (u32)(size_t)smem;
  const u32 tag32 = (u32)tag;
  const u32 oslot = WS_OG + (u32)(j * 9216);
  if (wave < 9) {
    Gemv1 g;
    gemv1_issue(g, p.wqA + 64 * j, p.x, t);
    gemv1_finish(g, red, wave, lane);
    STAMP(9);
  } else if (wave >= 12) {
    for (int it = 0; it < DMA_EARLY; ++it) {
      ckv_dma(p, lds0, b, s, wave - 12, lane, it);
      asm volatile("s_waitcnt vmcnt(1)" ::: "memory");
    }
  }
  STAMP(1);
  BAR();
  if (wave == 0) {
    f32x2 sq = {0.f, 0.f};
#pragma unroll
    for (int v = 0; v < 9; ++v) sq += *(const f32x2*)(red + v * 128 + 2 * lane);
    const u32 thi = (u32)(tag >> 32), tlo = (u32)tag & 0xffff0000u;
    st_sc1(rs, WS_QA + (u32)(((lane >> 5) * 4096 + 64 * j + (lane & 31) * 2) * 8),
           u32x4{tlo | bf1(sq[0]), thi, tlo | bf1(sq[1]), thi});
    STAMP(2);
  }
  if (wave < 9) {
    st_sc1(rs, oslot + t * 16, u32x4{0u, ~tag32, 0u, ~tag32});
  } else if (wave == 9) {
    poll_flags(p.ws, WS_AFLAG + ((j >> 2) + 16 * (lane & 7)) * 8, lane < 8, tag);
    poll_flags(p.ws, WS_F1FLAG + lane * 8, true, tag);
    STAMP(2);
  } else if (wave >= 12) {
#pragma unroll 4
    for (int it = DMA_EARLY; it < 32; ++it) { ckv_dma(p, lds0, b, s, wave - 12, lane, it); WAITV(7); }
  }
  BAR();
  {
    u32x4 v0[2], v1[2];
#pragma unroll
    for (int u = 0; u < 2; ++u) {
      const int n = t + 1024 * u;
      v0[u] = ld_sc1(rs, WS_QABS + (u32)((b * 2 + 0) * 32768 + n * 16));
      v1[u] = ld_sc1(rs, WS_QABS + (u32)((b * 2 + 1) * 32768 + n * 16));
    }
#pragma unroll
    for (int u = 0; u < 2; ++u) {
      const int n = t + 1024 * u;
      u32x4 o;
#pragma unroll
      for (int e = 0; e < 4; ++e) o[e] = pk2(bflo(v0[u][e]) + bflo(v1[u][e]), bfhi(v0[u][e]) + bfhi(v1[u][e]));
      const u32 h = n >> 6, c8 = n & 63;
      *(u32x4*)(smem + QOFF + (c8 >> 4) * 8192 + off_b(h, c8 & 15)) = o;
    }
    DRAIN();
  }
  STAMP(3);
  BAR();
  STAMP(7);
  if (lane == 0) ((GAS u64*)(p.ws + WS_DBG))[(256 * 16 + blockIdx.x * 16 + wave) * 16 + 0] = __builtin_amdgcn_s_memtime();
  const int l31 = lane & 31, hf = lane >> 5;
  float* stat = (float*)(smem + QOFF + 8192);
  f32x16 sacc;
  float Mh = 0.f;
  if (wave < 4) {
#pragma unroll
    for (int i = 0; i < 16; ++i) sacc[i] = 0.f;
    u32x4 av[2][4], bv[2][4];
#define S_LOAD(BT)                                                                              \
  _Pragma("unroll") for (int u = 0; u < 4; ++u) {                                               \
    const u32 ks = 4 * (BT) + u, cb = ks >> 3, ss = ks & 7;                                      \
    av[(BT) & 1][u] = *(const u32x4*)(smem + cb * 32768 + off_b(32 * wave + l31, 2 * ss + hf));  \
    bv[(BT) & 1][u] = *(const u32x4*)(smem + QOFF + cb * 8192 + off_b(l31, 2 * ss + hf));        \
  }
    S_LOAD(0)
#pragma unroll
    for (int bt = 0; bt < 8; ++bt) {
      if (bt < 7) { S_LOAD(bt + 1) }
      SCHED();
#pragma unroll
      for (int u = 0; u < 4; ++u) sacc = mfma32(av[bt & 1][u], bv[bt & 1][u], sacc);
      SCHED();
    }
    float mx = -INFINITY;
#pragma unroll
    for (int i = 0; i < 16; ++i) { sacc[i] *= C2; mx = fmaxf(mx, sacc[i]); }
    Mh = x32_max(mx);
  }
  STAMP(10);
  if (lane == 0) ((GAS u64*)(p.ws + WS_DBG))[(256 * 16 + blockIdx.x * 16 + wave) * 16 + 1] = __builtin_amdgcn_s_memtime();
  BAR();
  if (wave < 4 && lane < 32) stat[wave * 32 + lane] = Mh;
  BAR();
  if (wave < 4) {
    Mh = fmaxf(fmaxf(stat[l31], stat[32 + l31]), fmaxf(stat[64 + l31], stat[96 + l31]));
    float ls = 0.f;
#pragma unroll
    for (int i = 0; i < 16; ++i) { sacc[i] = ex2(sacc[i] - Mh); ls += sacc[i]; }
    ls = x32_sum(ls);
    if (lane < 32) stat[128 + wave * 32 + lane] = ls;
#pragma unroll
    for (int g = 0; g < 4; ++g) {
      const u32x2 pv = {pk2(sacc[4 * g], sacc[4 * g + 1]), pk2(sacc[4 * g + 2], sacc[4 * g + 3])};
      *(u32x2*)(smem + QOFF + off_b(l31, 4 * wave + g) + 8 * hf) = pv;
    }
  }
  BAR();
  STAMP(11);
  const float Lh = (stat[128 + l31] + stat[160 + l31]) + (stat[192 + l31] + stat[224 + l31]);
  {
    f32x16 oacc;
#pragma unroll
    for (int i = 0; i < 16; ++i) oacc[i] = 0.f;
    const u32 cb = wave >> 2, ct = wave & 3;
    const u32 blk = (lane >> 4) & 1, q4 = (lane & 15) >> 2, p4 = lane & 3;
    u32x4 av[8], bv[8];
#pragma unroll
    for (int ks = 0; ks < 8; ++ks) {
      av[ks] = tr_pair(smem, cb * 32768 + off_b(16 * ks + 8 * hf + q4, 4 * ct + 2 * blk + (p4 >> 1)) + 8 * (p4 & 1),
                             cb * 32768 + off_b(16 * ks + 8 * hf + 4 + q4, 4 * ct + 2 * blk + (p4 >> 1)) + 8 * (p4 & 1));
      bv[ks] = *(const u32x4*)(smem + QOFF + off_b(l31, 2 * ks + hf));
    }
    SCHED();
#pragma unroll
    for (int ks = 0; ks < 8; ++ks) oacc = mfma32(av[ks], bv[ks], oacc);
    const float li = 1.f / Lh;
    STAMP(12);
    BAR();
#pragma unroll
    for (int g = 0; g < 4; ++g) {
      const u32x2 ov = {pk2(oacc[4 * g] * li, oacc[4 * g + 1] * li), pk2(oacc[4 * g + 2] * li, oacc[4 * g + 3] * li)};
      *(u32x2*)(smem + 81920 + l31 * 1040 + 64 * wave + 16 * g + 8 * hf) = ov;
    }
  }
  BAR();
  STAMP(13);
#pragma unroll
  for (int u = 0; u < 2; ++u) {
    const int h = 2 * wave + u;
    const u32x4 v = *(const u32x4*)(smem + 81920 + h * 1040 + lane * 16);
    st_sc1(rs, WS_OLAT + (u32)(((h * 2 + b) * 32 + s) * 1024 + lane * 16), v);
  }
  if (t < 32) st8_sc1(p.ws, WS_ML + (u32)(((t * 2 + b) * 32 + s) * 8), ((u64)__float_as_uint(Lh) << 32) | __float_as_uint(Mh));
  STAMP(14);
  DRAIN();
  STAMP(15);
  BAR();
  if (t == 0) st8_sc1(p.ws, WS_F2FLAG2 + j * 8, tag);
  STAMP(4);
  if (wave >= 8) { wo_dma(p.woA + (size_t)(64 * j) * 576, lds0, wave - 8, lane); DRAIN(); }
  float* ag = (float*)(smem + QOFF);
  constexpr u32 ZIMG = QOFF + 4096;
  if (t < 264) {
    const f32x2 v = spin_pair(rs, WS_AG + (u32)(j * 8 * 66 * 8 + t * 16), tag32);
    *(f32x2*)(ag + 2 * t) = v;
  }
  STAMP(5);
  BAR();
  if (t < 128) {
    const int bb = t >> 6, d = t & 63;
    const float* a = ag + bb * 4 * 66;
    const float m0 = a[64], m1 = a[66 + 64], m2 = a[132 + 64], m3 = a[198 + 64];
    const float M = fmaxf(fmaxf(m0, m1), fmaxf(m2, m3));
    const float w0 = ex2(m0 - M), w1 = ex2(m1 - M), w2 = ex2(m2 - M), w3 = ex2(m3 - M);
    const float L = w0 * a[65] + w1 * a[66 + 65] + w2 * a[132 + 65] + w3 * a[198 + 65];
    const float z = (w0 * a[d] + w1 * a[66 + d] + w2 * a[132 + d] + w3 * a[198 + d]) / L;
    z_store(smem, ZIMG, bb, d, z);
  }
  BAR();
  oproj_mfma(smem, 0, ZIMG, rs, oslot, tag32, wave, lane);
  STAMP(6);
}

DI void role_F1(const Params& p, unsigned char* smem, __amdgpu_buffer_rsrc_t rs, u64 tag, int w) {
  const int t = threadIdx.x, lane = t & 63, wave = __builtin_amdgcn_readfirstlane(t >> 6);
  const int h = w & 31, dh = w >> 5;
  const int col0 = h * 128 + dh * 64;
  constexpr u32 UV_OFF = 0, WO_OFF = 65536, AI_OFF = 139264, SM_OFF = 155648;
  constexpr u32 ZIMG = SM_OFF + 2048;
  float* red = (float*)(smem + AI_OFF);
  unsigned char* qsh = smem + AI_OFF + 5120;
  float* qas = (float*)(smem + AI_OFF + 8192);
  float* part = (float*)(smem + SM_OFF);
  float* gsh = (float*)(smem + SM_OFF + 2560);
  float* fin = (float*)(smem + SM_OFF + 3072);
  const u32 lds0 = (u32)(size_t)smem;
  const u32 tag32 = (u32)tag;
  const u32 oslot = WS_OG + (u32)((64 + w) * 9216);
  u32x4 uk[4];
  const int ch = t & 7;
  if (wave < 9) {
    Gemv1 g;
    gemv1_issue(g, p.wqB + col0, p.x, t);
#pragma unroll
    for (int k = 0; k < 4; ++k) uk[k] = ldg16nt(p.wuk + (size_t)((t >> 3) + 128 * k) * 4096 + col0 + 8 * ch);
    STAMP(8);
    gemv1_finish(g, red, wave, lane);
    STAMP(9);
  } else {
#pragma unroll
    for (int k = 0; k < 4; ++k) uk[k] = ldg16nt(p.wuk + (size_t)((t >> 3) + 128 * k) * 4096 + col0 + 8 * ch);
    if (wave >= 14) {
      const u16* xr = p.x + (wave - 14) * 576;
      float sg = 0.f;
#pragma unroll
      for (int k = 0; k < 9; ++k) {
        const int mm = lane + 64 * k;
        sg = fmaf(__uint_as_float((u32)xr[mm] << 16), __uint_as_float((u32)p.gw[mm] << 16), sg);
      }
      sg += dpp<XOR1>(sg); sg += dpp<XOR2>(sg); sg += dpp<ROR4>(sg); sg += dpp<ROR8>(sg);
      sg = x32_sum(x16_sum(sg));
      if (lane == 0) gsh[wave - 14] = 1.f / (1.f + __expf(-sg));
    }
  }
  BAR();
  if (t < 16) *(u32x4*)(qsh + t * 16) = gemv1_sum(red, t);
  BAR();
  {
    const u32x4 q0 = *(const u32x4*)(qsh + ch * 16), q1 = *(const u32x4*)(qsh + 128 + ch * 16);
    float x[8];
#pragma unroll
    for (int k = 0; k < 4; ++k) { x[2 * k] = dot8(uk[k], q0); x[2 * k + 1] = dot8(uk[k], q1); }
    const int c = ch;
    TR_STEP(4, XOR7, 4) TR_STEP(2, XOR2, 2) TR_STEP(1, XOR1, 1)
    qas[(ch & 1) * 512 + (t >> 3) + 128 * (ch >> 1)] = x[0];
  }
  STAMP(10);
  BAR();
  if (wave < 2) {
    const f32x4 v0 = *(const f32x4*)(qas + t * 8), v1 = *(const f32x4*)(qas + t * 8 + 4);
    const u32x4 o = {pk2(v0[0], v0[1]), pk2(v0[2], v0[3]), pk2(v1[0], v1[1]), pk2(v1[2], v1[3])};
    st_sc1(rs, WS_QABS + (u32)((((t >> 6) * 2 + dh) * 32 + h) * 1024 + (t & 63) * 16), o);
    DRAIN();
  }
  BAR();
  if (t == 0) st8_sc1(p.ws, WS_F1FLAG + w * 8, tag);
  STAMP(1);
  if (wave < 8) {
    wo_dma(p.woB + (size_t)col0 * 576, lds0 + WO_OFF, wave, lane);
  } else {
#pragma unroll
    for (int it = 0; it < 8; ++it) {
      const int n = (wave - 8) * 8 + it;
      const u32 row = n * 8 + (lane >> 3);
      glds16(p.wuv + (size_t)row * 4096 + col0 + (((lane & 7) ^ wsw(row)) << 3), lds0 + UV_OFF + n * 1024);
    }
  }
  if (t < 576) st_sc1(rs, oslot + t * 16, u32x4{0u, ~tag32, 0u, ~tag32});
  DRAIN();
  BAR();
  if (wave == 0) {
    if (lane == 0) st8_sc1(p.ws, WS_F1FLAGC + w * 8, tag);
    poll_flags(p.ws, WS_F1FLAGC + lane * 8, true, tag);
    poll_flags(p.ws, WS_F2FLAG2 + lane * 8, true, tag);
  }
  STAMP(2);
  BAR();
  {
    const int bb = wave >> 3, sg = wave & 7;
    u32x4 pv[4];
#pragma unroll
    for (int u = 0; u < 4; ++u) pv[u] = ld_sc1(rs, WS_OLAT + (u32)(((h * 2 + bb) * 32 + 4 * sg + u) * 1024 + lane * 16));
    const u64 ml = ld8_sc1(p.ws, WS_ML + (u32)(((h * 2 + bb) * 32 + (lane & 31)) * 8));
    const float ms = __uint_as_float((u32)ml), ls = __uint_as_float((u32)(ml >> 32));
    float M = ms;
    M = fmaxf(M, dpp<XOR1>(M)); M = fmaxf(M, dpp<XOR2>(M)); M = fmaxf(M, dpp<ROR4>(M)); M = fmaxf(M, dpp<ROR8>(M));
    M = x16_max(M);
    const float wl = ls * ex2(ms - M);
    float L = wl;
    L += dpp<XOR1>(L); L += dpp<XOR2>(L); L += dpp<ROR4>(L); L += dpp<ROR8>(L);
    L = x16_sum(L);
    const float wn = wl / L;
    float a[8];
#pragma unroll
    for (int e = 0; e < 8; ++e) a[e] = 0.f;
#pragma unroll
    for (int u = 0; u < 4; ++u) {
      const float wu = __shfl(wn, 4 * sg + u);
#pragma unroll
      for (int e = 0; e < 8; ++e) a[e] = fmaf(wu, (e & 1) ? bfhi(pv[u][e >> 1]) : bflo(pv[u][e >> 1]), a[e]);
    }
    *(u32x4*)(smem + AI_OFF + (lane >> 4) * 4096 + off_b(wave, lane & 15)) =
        u32x4{pk2(a[0], a[1]), pk2(a[2], a[3]), pk2(a[4], a[5]), pk2(a[6], a[7])};
  }
  STAMP(3);
  BAR();
  {
    const int nt = wave & 3, kq = wave >> 2;
    u32x4 af[4], bf[4];
#pragma unroll
    for (int u = 0; u < 4; ++u) {
      af[u] = *(const u32x4*)(smem + AI_OFF + kq * 4096 + off_b(lane & 15, 4 * u + (lane >> 4)));
      bf[u] = wfrag(smem, UV_OFF, 128, 4 * kq + u, nt, lane);
    }
    f32x4 acc = {0.f, 0.f, 0.f, 0.f};
#pragma unroll
    for (int u = 0; u < 4; ++u) acc = mfma16(af[u], bf[u], acc);
    const float sv = x16_sum((acc[0] + acc[1]) + (acc[2] + acc[3]));
    if ((lane & 16) == 0) part[(kq * 2 + (lane >> 5)) * 64 + nt * 16 + (lane & 15)] = sv;
  }
  STAMP(12);
  BAR();
  if (t < 128) z_store(smem, ZIMG, t >> 6, t & 63, (part[t] + part[128 + t]) + (part[256 + t] + part[384 + t]));
  BAR();
  STAMP(13);
  oproj_mfma(smem, WO_OFF, ZIMG, rs, oslot, tag32, wave, lane);
  STAMP(4);
  if (t < 576) {
    const int pp = t / 9, i = t - pp * 9;
    const u32 off = WS_OG + (u32)(pp * 9216 + (9 * w + i) * 16);
    const f32x2 vb = spin_pair(rs, off + 64 * 9216, tag32);
    STAMP(5);
    const f32x2 va = spin_pair(rs, off, tag32);
    const float g0 = gsh[0], g1 = gsh[1];
    *(f32x2*)(fin + 2 * t) = f32x2{g0 * va[0] + (1.f - g0) * vb[0], g1 * va[1] + (1.f - g1) * vb[1]};
  }
  STAMP(6);
  BAR();
  if (t < 192) {
    const int o = t >> 3, k = t & 7;
    float sum = 0.f;
    if (o < 18) {
#pragma unroll
      for (int u = 0; u < 8; ++u) sum += fin[(k * 8 + u) * 18 + o];
    }
    sum += dpp<XOR1>(sum); sum += dpp<XOR2>(sum); sum += dpp<XOR7>(sum);
    if (k == 0 && o < 18) p.out[(o & 1) * 576 + 9 * w + (o >> 1)] = bf1(sum);
    if (w == 0 && t == 0) st8_sc1(p.ws, WS_EPOCH, tag);
  }
  STAMP(7);
}

__global__ void __launch_bounds__(1024, 4) fused_kernel(Params p) {
  __shared__ __attribute__((aligned(16))) unsigned char smem[LDS_BYTES];
  const u64 epoch = *(const __attribute__((address_space(4))) u64*)(p.ws + WS_EPOCH);
  STAMP(0);
  const u64 tag = mix64(epoch);
  const __amdgpu_buffer_rsrc_t rs = __builtin_amdgcn_make_buffer_rsrc(p.ws, 0, WS_END, 0x00020000);
  const int w = blockIdx.x;
  if (w < 64) role_F1(p, smem, rs, tag, w);
  else if (w < 128) role_F2(p, smem, rs, tag, w - 64);
  else role_A(p, smem, rs, tag, w - 128);
}

__global__ void spin_kernel(unsigned char* ws) {
  const u64* dbg = (const u64*)(ws + WS_DBG);
  __shared__ u64 smn[256], smx[256];
  const int w = threadIdx.x;
  u64 mn = ~0ull;
  for (int v = 0; v < 16; ++v) { const u64 x = dbg[(w * 16 + v) * 16]; mn = x < mn ? x : mn; }
  smn[w] = mn;
  __syncthreads();
  u64 t0 = ~0ull;
  for (int i = 0; i < 256; ++i) t0 = smn[i] < t0 ? smn[i] : t0;
  u64 mx = 0;
  if (w >= 0 && w < 256)
    for (int v = 0; v < 16; ++v) { const u64 x = dbg[(w * 16 + v) * 16 + 0]; if (x >= t0 && x < t0 + 100000 && x - t0 > mx) mx = x - t0; }
  smx[w] = mx;
  __syncthreads();
  u64 D = 0;
  for (int i = 0; i < 256; ++i) D = smx[i] > D ? smx[i] : D;
  const u64 s0 = __builtin_amdgcn_s_memrealtime();
  while (__builtin_amdgcn_s_memrealtime() - s0 < D * 10) __builtin_amdgcn_s_sleep(8);
}
}

extern "C" void kernel_launch(void* const* d_in, const int* in_sizes, int n_in,
                              void* d_out, int out_size, void* d_ws, size_t ws_size,
                              hipStream_t stream) {
  Params p;
  p.x = (const u16*)d_in[0];
  p.K = (const u16*)d_in[1];
  p.V = (const u16*)d_in[2];
  p.wqA = (const u16*)d_in[3];
  p.woA = (const u16*)d_in[4];
  p.ckv = (const u16*)d_in[6];
  p.wqB = (const u16*)d_in[7];
  p.wuk = (const u16*)d_in[8];
  p.wuv = (const u16*)d_in[9];
  p.woB = (const u16*)d_in[10];
  p.gw = (const u16*)d_in[11];
  p.out = (u16*)d_out;
  p.ws = (unsigned char*)d_ws;
  hipLaunchKernelGGL(fused_kernel, dim3(256), dim3(1024), 0, stream, p);
  hipLaunchKernelGGL(spin_kernel, dim3(1), dim3(256), 0, stream, p.ws);
}
```

```cpp
#define DBG_STAMPS
#include <hip/hip_runtime.h>
#include <hip/hip_bf16.h>
#include <stdint.h>

namespace {

typedef unsigned short u16;
typedef unsigned int u32;
typedef unsigned long long u64;
typedef u32 u32x4 __attribute__((ext_vector_type(4)));
typedef u32 u32x2 __attribute__((ext_vector_type(2)));
typedef float f32x4 __attribute__((ext_vector_type(4)));
typedef float f32x2 __attribute__((ext_vector_type(2)));
typedef float f32x16 __attribute__((ext_vector_type(16)));
typedef short s16x4 __attribute__((ext_vector_type(4)));
typedef __bf16 bf16x2 __attribute__((ext_vector_type(2)));
typedef __bf16 bf16x8 __attribute__((ext_vector_type(8)));
#define GAS __attribute__((address_space(1)))
#define LAS __attribute__((address_space(3)))
#define RLX_AGENT __ATOMIC_RELAXED, __HIP_MEMORY_SCOPE_AGENT
#define DI __device__ __forceinline__

constexpr u32 WS_EPOCH = 0;
constexpr u32 WS_AFLAG = 3072;
constexpr u32 WS_F1FLAGC = 4096;
constexpr u32 WS_QA = 8192;
constexpr u32 WS_QABS = 8192 + 65536;
constexpr u32 WS_ML = WS_QABS + 131072;
constexpr u32 WS_OLAT = WS_ML + 16384;
constexpr u32 WS_AG = WS_OLAT + 2097152;
constexpr u32 WS_OG = WS_AG + 64 * 8 * 66 * 8;
constexpr u32 WS_F1FLAG = WS_OG + 2 * 64 * 576 * 16;
constexpr u32 WS_F2FLAG2 = WS_F1FLAG + 32768;
constexpr u32 WS_END = WS_F2FLAG2 + 32768;
constexpr u32 WS_DBG = 8u << 20;

constexpr int LDS_BYTES = 163840;
constexpr u32 QOFF = 131072;
constexpr float C2 = 0.08838834764831845f * 1.4426950408889634f;
constexpr u32 SPIN_CAP = 1u << 18;
#define A_SLOW 3
#define A_SLOWD 1
#define DMA_EARLY 4
#define UK_DELAY 40
#define LATE_DMA 1200
#define POLL_SLEEP 4
#define F2_QDELAY 0
#define LATE_UV 1200
#define A_DEPTH 4
#define WAITV_(n) asm volatile("s_waitcnt vmcnt(" #n ")" ::: "memory")
#define WAITV(n) WAITV_(n)
#define AD1_2 1
#define AD1_3 2
#define AD1_4 3
#define AD1_5 4
#define AD1_6 5
#define AD1__(d) AD1_##d
#define AD1_(d) AD1__(d)

#define BAR() asm volatile("s_waitcnt lgkmcnt(0)\n\ts_barrier" ::: "memory")
#define DRAIN() asm volatile("s_waitcnt vmcnt(0)" ::: "memory")
#define SCHED() __builtin_amdgcn_sched_barrier(0)

struct Params {
  const u16 *x, *K, *V, *wqA, *woA, *ckv, *wqB, *wuk, *wuv, *woB, *gw;
  u16* out;
  unsigned char* ws;
};

DI float bflo(u32 v) { return __uint_as_float(v << 16); }
DI float bfhi(u32 v) { return __uint_as_float(v & 0xffff0000u); }
DI u32 pk2(float lo, float hi) {
  bf16x2 r; r[0] = (__bf16)lo; r[1] = (__bf16)hi; return __builtin_bit_cast(u32, r);
}
DI u16 bf1(float v) { const __bf16 r = (__bf16)v; return __builtin_bit_cast(u16, r); }
DI float dot2(u32 a, u32 b, float acc) {
  return __builtin_amdgcn_fdot2_f32_bf16(__builtin_bit_cast(bf16x2, a), __builtin_bit_cast(bf16x2, b), acc, false);
}
DI float dot8(u32x4 a, u32x4 b) {
  float d = dot2(a[0], b[0], 0.f); d = dot2(a[1], b[1], d); d = dot2(a[2], b[2], d); return dot2(a[3], b[3], d);
}
template <int CTRL> DI float dpp(float x) {
  return __builtin_bit_cast(float, __builtin_amdgcn_mov_dpp(__builtin_bit_cast(int, x), CTRL, 0xf, 0xf, true));
}
constexpr int XOR1 = 0xB1, XOR2 = 0x4E, XOR7 = 0x141, ROR8 = 0x128, ROR4 = 0x124;
DI float tr32(float a, float b) {
  auto s = __builtin_amdgcn_permlane32_swap(__float_as_uint(a), __float_as_uint(b), false, false);
  return __uint_as_float(s[0]) + __uint_as_float(s[1]);
}
DI float tr16(float a, float b) {
  auto s = __builtin_amdgcn_permlane16_swap(__float_as_uint(a), __float_as_uint(b), false, false);
  return __uint_as_float(s[0]) + __uint_as_float(s[1]);
}
DI float x16_max(float x) {
  auto s = __builtin_amdgcn_permlane16_swap(__float_as_uint(x), __float_as_uint(x), false, false);
  return fmaxf(__uint_as_float(s[0]), __uint_as_float(s[1]));
}
DI float x32_max(float x) {
  auto s = __builtin_amdgcn_permlane32_swap(__float_as_uint(x), __float_as_uint(x), false, false);
  return fmaxf(__uint_as_float(s[0]), __uint_as_float(s[1]));
}
DI float x16_sum(float x) { return tr16(x, x); }
DI float x32_sum(float x) { return tr32(x, x); }
DI float ex2(float x) { return __builtin_amdgcn_exp2f(x); }

DI u32x4 ldg16(const u16* p) { return *(const GAS u32x4*)p; }
DI u32x4 ldg16nt(const u16* p) { return __builtin_nontemporal_load((const GAS u32x4*)p); }
DI u32x4 ld_sc1(__amdgpu_buffer_rsrc_t rs, u32 off) { return __builtin_amdgcn_raw_buffer_load_b128(rs, off, 0, 16); }
DI void st_sc1(__amdgpu_buffer_rsrc_t rs, u32 off, u32x4 v) { __builtin_amdgcn_raw_buffer_store_b128(v, rs, off, 0, 16); }
DI u64 ld8_sc1(unsigned char* ws, u32 off) { return __hip_atomic_load((GAS u64*)(ws + off), RLX_AGENT); }
DI void st8_sc1(unsigned char* ws, u32 off, u64 v) { __hip_atomic_store((GAS u64*)(ws + off), v, RLX_AGENT); }

DI u64 mix64(u64 e) {
  u64 z = e + 0x9E3779B97F4A7C15ull;
  z = (z ^ (z >> 30)) * 0xBF58476D1CE4E5B9ull;
  z = (z ^ (z >> 27)) * 0x94D049BB133111EBull;
  return z ^ (z >> 31);
}
DI void poll_flags(unsigned char* ws, u32 off, bool active, u64 tag) {
  for (u32 spins = 0; spins < SPIN_CAP; ++spins) {
    bool ok = true;
    if (active) ok = (ld8_sc1(ws, off) == tag);
    if (__all(ok)) break;
    __builtin_amdgcn_s_sleep(POLL_SLEEP);
  }
  __builtin_amdgcn_fence(__ATOMIC_ACQUIRE, "wavefront");
}
DI f32x2 spin_pair(__amdgpu_buffer_rsrc_t rs, u32 off, u32 tag32) {
  u32x4 v;
  for (u32 spins = 0; spins < SPIN_CAP; ++spins) {
    asm volatile("" ::: "memory");
    v = ld_sc1(rs, off);
    bool ok = (v[1] == tag32) & (v[3] == tag32);
    if (__all(ok)) break;
  }
  f32x2 r; r[0] = __uint_as_float(v[0]); r[1] = __uint_as_float(v[2]); return r;
}
DI void glds16(const void* gsrc, u32 lds_dst) {
  u32 keep;
  asm volatile("s_mov_b32 %0, m0\n\ts_mov_b32 m0, %2\n\ts_nop 0\n\tglobal_load_lds_dwordx4 %1, off nt\n\ts_mov_b32 m0, %0"
               : "=&s"(keep) : "v"(gsrc), "s"(lds_dst) : "memory");
}
DI u32 off_b(u32 row, u32 ch) { return 256u * row + 16u * (ch ^ (((row & 3) << 2) | ((row >> 2) & 3))); }
DI u32 wsw(u32 row) { return (((row >> 1) & 1) | (((row >> 3) & 1) << 1)) << 1; }
DI f32x4 mfma16(u32x4 a, u32x4 b, f32x4 c) {
  return __builtin_amdgcn_mfma_f32_16x16x32_bf16(__builtin_bit_cast(bf16x8, a), __builtin_bit_cast(bf16x8, b), c, 0, 0, 0);
}
DI f32x16 mfma32(u32x4 a, u32x4 b, f32x16 c) {
  return __builtin_amdgcn_mfma_f32_32x32x16_bf16(__builtin_bit_cast(bf16x8, a), __builtin_bit_cast(bf16x8, b), c, 0, 0, 0);
}
DI u32x4 tr_pair(const unsigned char* smem, u32 a0, u32 a1) {
  const s16x4 lo = __builtin_amdgcn_ds_read_tr16_b64_v4i16((LAS s16x4*)(smem + a0));
  const s16x4 hi = __builtin_amdgcn_ds_read_tr16_b64_v4i16((LAS s16x4*)(smem + a1));
  return __builtin_bit_cast(u32x4, __builtin_shufflevector(lo, hi, 0, 1, 2, 3, 4, 5, 6, 7));
}
DI u32x4 wfrag(const unsigned char* smem, u32 img, u32 stride, u32 ks, u32 ct, int lane) {
  const u32 g = lane >> 4, q = (lane & 15) >> 2, pp = lane & 3;
  const u32 r0 = 32 * ks + 8 * g + q, r1 = r0 + 4;
  const u32 ch = 2 * ct + (pp >> 1);
  return tr_pair(smem, img + r0 * stride + ((ch ^ wsw(r0)) << 4) + 8 * (pp & 1),
                       img + r1 * stride + ((ch ^ wsw(r1)) << 4) + 8 * (pp & 1));
}

#define STAMP(i) do { if ((threadIdx.x & 63) == 0) ((GAS u64*)(p.ws + WS_DBG))[(blockIdx.x * 16 + (threadIdx.x >> 6)) * 16 + (i)] = __builtin_amdgcn_s_memrealtime(); } while (0)

struct Gemv1 { u32x4 w[8]; u32x4 x0, x1; };
DI void gemv1_issue(Gemv1& g, const u16* W, const u16* x, int t) {
  const int rg = t >> 3, ch = t & 7;
  const u16* wp = W + (size_t)(8 * rg) * 4096 + 8 * ch;
  g.x0 = ldg16(x + 8 * rg);
  g.x1 = ldg16(x + 576 + 8 * rg);
#pragma unroll
  for (int k = 0; k < 8; ++k) g.w[k] = ldg16nt(wp + (size_t)k * 4096);
}
DI void gemv1_finish(Gemv1& g, float* red  , int wave, int lane) {
  float a0[8], a1[8];
#pragma unroll
  for (int e = 0; e < 8; ++e) { a0[e] = 0.f; a1[e] = 0.f; }
#pragma unroll
  for (int k = 0; k < 8; ++k) {
    const float xa = (k & 1) ? bfhi(g.x0[k >> 1]) : bflo(g.x0[k >> 1]);
    const float xb = (k & 1) ? bfhi(g.x1[k >> 1]) : bflo(g.x1[k >> 1]);
#pragma unroll
    for (int e = 0; e < 8; ++e) {
      const float wf = (e & 1) ? bfhi(g.w[k][e >> 1]) : bflo(g.w[k][e >> 1]);
      a0[e] = fmaf(xa, wf, a0[e]); a1[e] = fmaf(xb, wf, a1[e]);
    }
  }
  float v[8], u[4], wv[2];
#pragma unroll
  for (int e = 0; e < 8; ++e) v[e] = tr32(a0[e], a1[e]);
#pragma unroll
  for (int e = 0; e < 4; ++e) u[e] = tr16(v[e], v[e + 4]);
#pragma unroll
  for (int e = 0; e < 2; ++e) {
    const float t1 = u[e] + dpp<ROR8>(u[e]), t2 = u[e + 2] + dpp<ROR8>(u[e + 2]);
    wv[e] = (lane & 8) ? t2 : t1;
  }
  *(f32x2*)(red + wave * 128 + (lane >> 5) * 64 + (lane & 7) * 8 + ((lane >> 3) & 1) * 2 + ((lane >> 4) & 1) * 4) = f32x2{wv[0], wv[1]};
}
DI u32x4 gemv1_sum(const float* red, int t) {
  f32x4 s0 = {0.f, 0.f, 0.f, 0.f}, s1 = {0.f, 0.f, 0.f, 0.f};
  const float* r = red + (t >> 3) * 64 + (t & 7) * 8;
#pragma unroll
  for (int v = 0; v < 9; ++v) { s0 += *(const f32x4*)(r + v * 128); s1 += *(const f32x4*)(r + v * 128 + 4); }
  return u32x4{pk2(s0[0], s0[1]), pk2(s0[2], s0[3]), pk2(s1[0], s1[1]), pk2(s1[2], s1[3])};
}
DI void wo_dma(const u16* W, u32 lds_img, int wv, int lane) {
#pragma unroll 3
  for (int it = 0; it < 9; ++it) {
    const int n = wv * 9 + it;
    const u32 P = n * 1024 + lane * 16, row = P / 1152, cpos = (P - row * 1152) >> 4;
    glds16(W + (size_t)row * 576 + ((cpos ^ wsw(row)) << 3), lds_img + n * 1024);
  }
}
DI void oproj_mfma(const unsigned char* smem, u32 img, u32 zimg, __amdgpu_buffer_rsrc_t rs, u32 slot, u32 tag32, int wave, int lane) {
  u32x4 a[2];
#pragma unroll
  for (int ks = 0; ks < 2; ++ks) {
    a[ks] = u32x4{0u, 0u, 0u, 0u};
    if ((lane & 15) < 4) a[ks] = *(const u32x4*)(smem + zimg + (lane & 15) * 128 + (32 * ks + 8 * (lane >> 4)) * 2);
  }
  const int nt = wave < 4 ? 3 : 2;
  u32x4 bfr[3][2];
#pragma unroll
  for (int i = 0; i < 3; ++i)
    if (i < nt) {
#pragma unroll
      for (int ks = 0; ks < 2; ++ks) bfr[i][ks] = wfrag(smem, img, 1152, ks, wave + 16 * i, lane);
    }
#pragma unroll
  for (int i = 0; i < 3; ++i)
    if (i < nt) {
      f32x4 acc = {0.f, 0.f, 0.f, 0.f};
      acc = mfma16(a[0], bfr[i][0], acc);
      acc = mfma16(a[1], bfr[i][1], acc);
      if (lane < 16)
        st_sc1(rs, slot + (u32)(((wave + 16 * i) * 16 + lane) * 16),
               u32x4{__float_as_uint(acc[0] + acc[2]), tag32, __float_as_uint(acc[1] + acc[3]), tag32});
    }
}
DI void z_store(unsigned char* smem, u32 zimg, int b, int d, float z) {
  const u16 hi = bf1(z);
  const u16 lo = bf1(z - __uint_as_float((u32)hi << 16));
  *(u16*)(smem + zimg + b * 128 + d * 2) = hi;
  *(u16*)(smem + zimg + (2 + b) * 128 + d * 2) = lo;
}

#define TR_STEP(HALF, CTRL, BIT)                                                          \
  _Pragma("unroll") for (int h_ = 0; h_ < (HALF); ++h_) {                                 \
    const float lo_ = x[h_], hi_ = x[h_ + (HALF)];                                        \
    const float t1_ = lo_ + dpp<CTRL>(lo_), t2_ = hi_ + dpp<CTRL>(hi_);                   \
    x[h_] = (c & (BIT)) ? t2_ : t1_;                                                      \
  }

constexpr int NLA = 16;
constexpr int NSP = 4;
DI void ag_clear(__amdgpu_buffer_rsrc_t rs, int hkv, int bs, u32 ntag, int lane) {
#pragma unroll
  for (int k = 0; k < 3; ++k) {
    const int n = lane + 64 * k;
    if (n < 132) {
      const int seg = n / 33, pos = n - seg * 33;
      st_sc1(rs, WS_AG + (u32)((((hkv * 4 + seg) * 8 + bs) * 66 + 2 * pos) * 8), u32x4{0u, ntag, 0u, ntag});
    }
  }
}
DI void q_fetch(__amdgpu_buffer_rsrc_t rs, unsigned char* qs, int b, int hkv, u64 tag, int lane) {
  const u32 thi = (u32)(tag >> 32), tlo = (u32)tag & 0xffff0000u;
  const u32 off = WS_QA + (u32)((b * 4096 + hkv * 256 + lane * 4) * 8);
  u32x4 g0, g1;
  for (u32 spins = 0; spins < SPIN_CAP; ++spins) {
    asm volatile("" ::: "memory");
    g0 = ld_sc1(rs, off); g1 = ld_sc1(rs, off + 16);
    const bool ok = (g0[1] == thi) & (g0[3] == thi) & (g1[1] == thi) & (g1[3] == thi) &
                    ((g0[0] & 0xffff0000u) == tlo) & ((g0[2] & 0xffff0000u) == tlo) &
                    ((g1[0] & 0xffff0000u) == tlo) & ((g1[2] & 0xffff0000u) == tlo);
    if (__all(ok)) break;
    __builtin_amdgcn_s_sleep(1);
  }
  *(u32x2*)(qs + lane * 8) = u32x2{(g0[0] & 0xffffu) | (g0[2] << 16), (g1[0] & 0xffffu) | (g1[2] << 16)};
}
template <int NL>
DI void gqa_wave(u32x4 (&kv)[NL], const u16* Vp, const unsigned char* qs, float* pw, float* mrg, float* mls, int wave, int lane) {
  constexpr int NG = (NL + 7) / 8, NLP = NL <= 4 ? 4 : 8 * NG;
  const int r = lane >> 4, c = lane & 15;
  const u32x4 q0 = *(const u32x4*)(qs + c * 16), q1 = *(const u32x4*)(qs + 256 + c * 16);
  float sc[NG];
#pragma unroll
  for (int G = 0; G < NG; ++G) {
    float x[16];
#pragma unroll
    for (int ii = 0; ii < 8; ++ii) {
      const int i = 8 * G + ii;
      if (i < NL) {
        x[2 * ii] = dot8(kv[i], q0);
        x[2 * ii + 1] = dot8(kv[i], q1);
        SCHED();
        kv[i] = ldg16nt(Vp + i * 512);
        SCHED();
      } else { x[2 * ii] = 0.f; x[2 * ii + 1] = 0.f; }
    }
    TR_STEP(8, ROR8, 8) TR_STEP(4, XOR7, 4) TR_STEP(2, XOR2, 2) TR_STEP(1, XOR1, 1)
    sc[G] = (8 * G + (c >> 1) < NL) ? x[0] * C2 : -INFINITY;
  }
  float m = sc[0];
#pragma unroll
  for (int G = 1; G < NG; ++G) m = fmaxf(m, sc[G]);
  m = fmaxf(m, dpp<XOR2>(m)); m = fmaxf(m, dpp<ROR4>(m)); m = fmaxf(m, dpp<ROR8>(m));
  m = x32_max(x16_max(m));
  float l = 0.f;
#pragma unroll
  for (int G = 0; G < NG; ++G) {
    const float pe = ex2(sc[G] - m);
    l += pe;
    if (16 * G + c < 2 * NLP) pw[r * 2 * NLP + 16 * G + c] = pe;
  }
  l += dpp<XOR2>(l); l += dpp<ROR4>(l); l += dpp<ROR8>(l);
  l = x32_sum(x16_sum(l));
  float a0[8], a1[8];
#pragma unroll
  for (int e = 0; e < 8; ++e) { a0[e] = 0.f; a1[e] = 0.f; }
#pragma unroll
  for (int i2 = 0; i2 < (NL + 1) / 2; ++i2) {
    const f32x4 pp = *(const f32x4*)(pw + r * 2 * NLP + i2 * 4);
#pragma unroll
    for (int ii = 0; ii < 2; ++ii) {
      const int i = 2 * i2 + ii;
      if (i < NL) {
        const float pa = pp[2 * ii], pb = pp[2 * ii + 1];
#pragma unroll
        for (int e = 0; e < 8; ++e) {
          const float vf = (e & 1) ? bfhi(kv[i][e >> 1]) : bflo(kv[i][e >> 1]);
          a0[e] = fmaf(pa, vf, a0[e]); a1[e] = fmaf(pb, vf, a1[e]);
        }
      }
    }
  }
  float v[8];
#pragma unroll
  for (int e = 0; e < 8; ++e) v[e] = tr32(a0[e], a1[e]);
  f32x4 u;
#pragma unroll
  for (int e = 0; e < 4; ++e) u[e] = tr16(v[e], v[e + 4]);
  *(f32x4*)(mrg + wave * 256 + (lane >> 5) * 128 + c * 8 + ((lane >> 4) & 1) * 4) = u;
  if (lane < 2) *(f32x2*)(mls + wave * 4 + lane * 2) = f32x2{m, l};
}
DI void gqa_merge(unsigned char* ws, const float* mrg, const float* mls, int hkv, int bs, u32 tag32, int t) {
  const int g = t >> 7, d = t & 127;
  float mv[16], M = -INFINITY;
#pragma unroll
  for (int v = 0; v < 16; ++v) { mv[v] = mls[v * 4 + g * 2]; M = fmaxf(M, mv[v]); }
  float L = 0.f, A = 0.f;
#pragma unroll
  for (int v = 0; v < 16; ++v) {
    const float w = ex2(mv[v] - M);
    L = fmaf(w, mls[v * 4 + g * 2 + 1], L);
    A = fmaf(w, mrg[v * 256 + t], A);
  }
  const u32 slot = WS_AG + (u32)((((hkv * 2 + g) * 2 + (d >> 6)) * 8 + bs) * 66 * 8);
  st8_sc1(ws, slot + (d & 63) * 8, ((u64)tag32 << 32) | __float_as_uint(A));
  if ((d & 63) < 2) st8_sc1(ws, slot + (64 + (d & 1)) * 8, ((u64)tag32 << 32) | __float_as_uint((d & 1) ? L : M));
}

DI void role_A(const Params& p, unsigned char* smem, __amdgpu_buffer_rsrc_t rs, u64 tag, int a) {
  const int t = threadIdx.x, lane = t & 63, wave = __builtin_amdgcn_readfirstlane(t >> 6);
  const int hkv = a & 15, b = (a >> 4) & 1, s = a >> 5;
  const size_t base = ((size_t)(b * 16 + hkv) * 4096 + s * (64 * NLA) + wave * (4 * NLA) + (lane >> 4)) * 128 + (lane & 15) * 8;
  const u16* Kp = p.K + base;
  unsigned char* qs = smem;
  float* pw = (float*)(smem + 1024) + wave * 128;
  float* mrg = (float*)(smem + 1024 + 8192);
  float* mls = mrg + 16 * 256;
  u32x4 kv[NLA];
  if (wave == 0) {
    ag_clear(rs, hkv, b * 4 + s, ~(u32)tag, lane);
    DRAIN();
    if (lane == 0) st8_sc1(p.ws, WS_AFLAG + a * 8, tag);
    q_fetch(rs, qs, b, hkv, tag, lane);
    STAMP(1);
    BAR();
#pragma unroll
    for (int i = 0; i < NLA; ++i) kv[i] = ldg16nt(Kp + i * 512);
  } else {
#pragma unroll
    for (int i = 0; i < NLA; ++i) {
      kv[i] = ldg16nt(Kp + i * 512);
      if (i < A_SLOW) {
        if (A_SLOWD == 1) asm volatile("s_waitcnt vmcnt(0)" ::: "memory");
        else if (i >= 1) asm volatile("s_waitcnt vmcnt(1)" ::: "memory");
      } else if (i >= A_DEPTH - 1 && i < NLA - 1) WAITV(AD1_(A_DEPTH));
    }
    STAMP(1);
    BAR();
  }
  STAMP(2);
  gqa_wave<NLA>(kv, p.V + base, qs, pw, mrg, mls, wave, lane);
  STAMP(3);
  BAR();
  if (t < 256) gqa_merge(p.ws, mrg, mls, hkv, b * 4 + s, (u32)tag, t);
  STAMP(4);
}

DI void ckv_dma(const Params& p, u32 lds0, int b, int s, int w4, int lane, int it) {
  const int n = it * 4 + w4;
  const u32 row = 4 * (n >> 2) + (lane >> 4), cb = n & 3;
  const u32 ch = (lane & 15) ^ (((row & 3) << 2) | ((row >> 2) & 3));
  glds16(p.ckv + ((size_t)(b * 4096 + s * 128 + row) * 512 + cb * 128 + ch * 8), lds0 + cb * 32768 + (n >> 2) * 1024);
}
DI void role_F2(const Params& p, unsigned char* smem, __amdgpu_buffer_rsrc_t rs, u64 tag, int j) {
  const int t = threadIdx.x, lane = t & 63, wave = __builtin_amdgcn_readfirstlane(t >> 6);
  const int b = j & 1, s = j >> 1;
  float* red = (float*)(smem + QOFF);
  const u32 lds0 = (u32)(size_t)smem;
  const u32 tag32 = (u32)tag;
  const u32 oslot = WS_OG + (u32)(j * 9216);
  if (wave < 9) {
    Gemv1 g;
    gemv1_issue(g, p.wqA + 64 * j, p.x, t);
    gemv1_finish(g, red, wave, lane);
    STAMP(9);
  } else if (wave >= 12) {
    for (int it = 0; it < DMA_EARLY; ++it) {
      ckv_dma(p, lds0, b, s, wave - 12, lane, it);
      if (F2_QDELAY) WAITV(7); else WAITV(1);
    }
  }
  STAMP(1);
  BAR();
  if (wave == 0) {
    f32x2 sq = {0.f, 0.f};
#pragma unroll
    for (int v = 0; v < 9; ++v) sq += *(const f32x2*)(red + v * 128 + 2 * lane);
    const u32 thi = (u32)(tag >> 32), tlo = (u32)tag & 0xffff0000u;
    st_sc1(rs, WS_QA + (u32)(((lane >> 5) * 4096 + 64 * j + (lane & 31) * 2) * 8),
           u32x4{tlo | bf1(sq[0]), thi, tlo | bf1(sq[1]), thi});
    STAMP(2);
  }
  BAR();
  if (wave < 9) {
    st_sc1(rs, oslot + t * 16, u32x4{0u, ~tag32, 0u, ~tag32});
  } else if (wave == 9) {
    poll_flags(p.ws, WS_AFLAG + ((j >> 2) + 16 * (lane & 7)) * 8, lane < 8, tag);
  } else if (wave >= 12) {
#pragma unroll 4
    for (int it = DMA_EARLY; it < 32; ++it) { ckv_dma(p, lds0, b, s, wave - 12, lane, it); WAITV(7); }
  }
  {
    poll_flags(p.ws, WS_F1FLAG + j * 512 + (wave + 16 * (lane & 3)) * 8, lane < 4, tag);
    u32x4 v0[2], v1[2];
#pragma unroll
    for (int u = 0; u < 2; ++u) {
      const int n = t + 1024 * u;
      v0[u] = ld_sc1(rs, WS_QABS + (u32)((b * 2 + 0) * 32768 + n * 16));
      v1[u] = ld_sc1(rs, WS_QABS + (u32)((b * 2 + 1) * 32768 + n * 16));
    }
#pragma unroll
    for (int u = 0; u < 2; ++u) {
      const int n = t + 1024 * u;
      u32x4 o;
#pragma unroll
      for (int e = 0; e < 4; ++e) o[e] = pk2(bflo(v0[u][e]) + bflo(v1[u][e]), bfhi(v0[u][e]) + bfhi(v1[u][e]));
      const u32 h = n >> 6, c8 = n & 63;
      *(u32x4*)(smem + QOFF + (c8 >> 4) * 8192 + off_b(h, c8 & 15)) = o;
    }
    DRAIN();
  }
  STAMP(3);
  BAR();
  STAMP(7);
  if (lane == 0) ((GAS u64*)(p.ws + WS_DBG))[(256 * 16 + blockIdx.x * 16 + wave) * 16 + 0] = __builtin_amdgcn_s_memtime();
  const int l31 = lane & 31, hf = lane >> 5;
  float* stat = (float*)(smem + QOFF + 8192);
  f32x16 sacc;
  float Mh = 0.f;
  if (wave < 4) {
#pragma unroll
    for (int i = 0; i < 16; ++i) sacc[i] = 0.f;
    u32x4 av[2][4], bv[2][4];
#define S_LOAD(BT)                                                                              \
  _Pragma("unroll") for (int u = 0; u < 4; ++u) {                                               \
    const u32 ks = 4 * (BT) + u, cb = ks >> 3, ss = ks & 7;                                      \
    av[(BT) & 1][u] = *(const u32x4*)(smem + cb * 32768 + off_b(32 * wave + l31, 2 * ss + hf));  \
    bv[(BT) & 1][u] = *(const u32x4*)(smem + QOFF + cb * 8192 + off_b(l31, 2 * ss + hf));        \
  }
    S_LOAD(0)
#pragma unroll
    for (int bt = 0; bt < 8; ++bt) {
      if (bt < 7) { S_LOAD(bt + 1) }
      SCHED();
#pragma unroll
      for (int u = 0; u < 4; ++u) sacc = mfma32(av[bt & 1][u], bv[bt & 1][u], sacc);
      SCHED();
    }
    float mx = -INFINITY;
#pragma unroll
    for (int i = 0; i < 16; ++i) { sacc[i] *= C2; mx = fmaxf(mx, sacc[i]); }
    Mh = x32_max(mx);
  }
  STAMP(10);
  if (lane == 0) ((GAS u64*)(p.ws + WS_DBG))[(256 * 16 + blockIdx.x * 16 + wave) * 16 + 1] = __builtin_amdgcn_s_memtime();
  BAR();
  if (wave < 4 && lane < 32) stat[wave * 32 + lane] = Mh;
  BAR();
  if (wave < 4) {
    Mh = fmaxf(fmaxf(stat[l31], stat[32 + l31]), fmaxf(stat[64 + l31], stat[96 + l31]));
    float ls = 0.f;
#pragma unroll
    for (int i = 0; i < 16; ++i) { sacc[i] = ex2(sacc[i] - Mh); ls += sacc[i]; }
    ls = x32_sum(ls);
    if (lane < 32) stat[128 + wave * 32 + lane] = ls;
#pragma unroll
    for (int g = 0; g < 4; ++g) {
      const u32x2 pv = {pk2(sacc[4 * g], sacc[4 * g + 1]), pk2(sacc[4 * g + 2], sacc[4 * g + 3])};
      *(u32x2*)(smem + QOFF + off_b(l31, 4 * wave + g) + 8 * hf) = pv;
    }
  }
  BAR();
  STAMP(11);
  const float Lh = (stat[128 + l31] + stat[160 + l31]) + (stat[192 + l31] + stat[224 + l31]);
  {
    f32x16 oacc;
#pragma unroll
    for (int i = 0; i < 16; ++i) oacc[i] = 0.f;
    const u32 cb = wave >> 2, ct = wave & 3;
    const u32 blk = (lane >> 4) & 1, q4 = (lane & 15) >> 2, p4 = lane & 3;
    u32x4 av[8], bv[8];
#pragma unroll
    for (int ks = 0; ks < 8; ++ks) {
      av[ks] = tr_pair(smem, cb * 32768 + off_b(16 * ks + 8 * hf + q4, 4 * ct + 2 * blk + (p4 >> 1)) + 8 * (p4 & 1),
                             cb * 32768 + off_b(16 * ks + 8 * hf + 4 + q4, 4 * ct + 2 * blk + (p4 >> 1)) + 8 * (p4 & 1));
      bv[ks] = *(const u32x4*)(smem + QOFF + off_b(l31, 2 * ks + hf));
    }
    SCHED();
#pragma unroll
    for (int ks = 0; ks < 8; ++ks) oacc = mfma32(av[ks], bv[ks], oacc);
    const float li = 1.f / Lh;
    STAMP(12);
    BAR();
#pragma unroll
    for (int g = 0; g < 4; ++g) {
      const u32x2 ov = {pk2(oacc[4 * g] * li, oacc[4 * g + 1] * li), pk2(oacc[4 * g + 2] * li, oacc[4 * g + 3] * li)};
      *(u32x2*)(smem + 81920 + l31 * 1040 + 64 * wave + 16 * g + 8 * hf) = ov;
    }
  }
  BAR();
  STAMP(13);
#pragma unroll
  for (int u = 0; u < 2; ++u) {
    const int h = 2 * wave + u;
    const u32x4 v = *(const u32x4*)(smem + 81920 + h * 1040 + lane * 16);
    st_sc1(rs, WS_OLAT + (u32)(((h * 2 + b) * 32 + s) * 1024 + lane * 16), v);
  }
  if (t < 32) st8_sc1(p.ws, WS_ML + (u32)(((t * 2 + b) * 32 + s) * 8), ((u64)__float_as_uint(Lh) << 32) | __float_as_uint(Mh));
  STAMP(14);
  DRAIN();
  STAMP(15);
  BAR();
  if (t < 64) st8_sc1(p.ws, WS_F2FLAG2 + t * 512 + j * 8, tag);
  STAMP(4);
  if (wave >= 8) { wo_dma(p.woA + (size_t)(64 * j) * 576, lds0, wave - 8, lane); DRAIN(); }
  float* ag = (float*)(smem + QOFF);
  constexpr u32 ZIMG = QOFF + 4096;
  if (t < 264) {
    const f32x2 v = spin_pair(rs, WS_AG + (u32)(j * 8 * 66 * 8 + t * 16), tag32);
    *(f32x2*)(ag + 2 * t) = v;
  }
  STAMP(5);
  BAR();
  if (t < 128) {
    const int bb = t >> 6, d = t & 63;
    const float* a = ag + bb * 4 * 66;
    float M = a[64];
#pragma unroll
    for (int u = 1; u < 4; ++u) M = fmaxf(M, a[u * 66 + 64]);
    float L = 0.f, z = 0.f;
#pragma unroll
    for (int u = 0; u < 4; ++u) { const float wu = ex2(a[u * 66 + 64] - M); L = fmaf(wu, a[u * 66 + 65], L); z = fmaf(wu, a[u * 66 + d], z); }
    z /= L;
    z_store(smem, ZIMG, bb, d, z);
  }
  BAR();
  oproj_mfma(smem, 0, ZIMG, rs, oslot, tag32, wave, lane);
  STAMP(6);
}

DI void role_F1(const Params& p, unsigned char* smem, __amdgpu_buffer_rsrc_t rs, u64 tag, int w) {
  const int t = threadIdx.x, lane = t & 63, wave = __builtin_amdgcn_readfirstlane(t >> 6);
  const u64 t_start = __builtin_amdgcn_s_memrealtime();
  const int h = w & 31, dh = w >> 5;
  const int col0 = h * 128 + dh * 64;
  constexpr u32 UV_OFF = 0, WO_OFF = 65536, AI_OFF = 139264, SM_OFF = 155648;
  constexpr u32 ZIMG = SM_OFF + 2048;
  float* red = (float*)(smem + AI_OFF);
  unsigned char* qsh = smem + AI_OFF + 5120;
  float* qas = (float*)(smem + AI_OFF + 8192);
  float* part = (float*)(smem + SM_OFF);
  float* gsh = (float*)(smem + SM_OFF + 2560);
  float* fin = (float*)(smem + SM_OFF + 3072);
  const u32 lds0 = (u32)(size_t)smem;
  const u32 tag32 = (u32)tag;
  const u32 oslot = WS_OG + (u32)((64 + w) * 9216);
  u32x4 uk[4];
  const int ch = t & 7;
  if (wave < 9) {
    Gemv1 g;
    gemv1_issue(g, p.wqB + col0, p.x, t);
    WAITV(4);
#pragma unroll
    for (int k = 0; k < 4; ++k) uk[k] = ldg16nt(p.wuk + (size_t)((t >> 3) + 128 * k) * 4096 + col0 + 8 * ch);
    STAMP(8);
    gemv1_finish(g, red, wave, lane);
    STAMP(9);
  } else {
    __builtin_amdgcn_s_sleep(UK_DELAY);
#pragma unroll
    for (int k = 0; k < 4; ++k) uk[k] = ldg16nt(p.wuk + (size_t)((t >> 3) + 128 * k) * 4096 + col0 + 8 * ch);
    if (wave >= 14) {
      const u16* xr = p.x + (wave - 14) * 576;
      float sg = 0.f;
#pragma unroll
      for (int k = 0; k < 9; ++k) {
        const int mm = lane + 64 * k;
        sg = fmaf(__uint_as_float((u32)xr[mm] << 16), __uint_as_float((u32)p.gw[mm] << 16), sg);
      }
      sg += dpp<XOR1>(sg); sg += dpp<XOR2>(sg); sg += dpp<ROR4>(sg); sg += dpp<ROR8>(sg);
      sg = x32_sum(x16_sum(sg));
      if (lane == 0) gsh[wave - 14] = 1.f / (1.f + __expf(-sg));
    }
  }
  BAR();
  if (t < 16) *(u32x4*)(qsh + t * 16) = gemv1_sum(red, t);
  BAR();
  {
    const u32x4 q0 = *(const u32x4*)(qsh + ch * 16), q1 = *(const u32x4*)(qsh + 128 + ch * 16);
    float x[8];
#pragma unroll
    for (int k = 0; k < 4; ++k) { x[2 * k] = dot8(uk[k], q0); x[2 * k + 1] = dot8(uk[k], q1); }
    const int c = ch;
    TR_STEP(4, XOR7, 4) TR_STEP(2, XOR2, 2) TR_STEP(1, XOR1, 1)
    qas[(ch & 1) * 512 + (t >> 3) + 128 * (ch >> 1)] = x[0];
  }
  STAMP(10);
  BAR();
  if (wave < 2) {
    const f32x4 v0 = *(const f32x4*)(qas + t * 8), v1 = *(const f32x4*)(qas + t * 8 + 4);
    const u32x4 o = {pk2(v0[0], v0[1]), pk2(v0[2], v0[3]), pk2(v1[0], v1[1]), pk2(v1[2], v1[3])};
    st_sc1(rs, WS_QABS + (u32)((((t >> 6) * 2 + dh) * 32 + h) * 1024 + (t & 63) * 16), o);
    DRAIN();
  }
  BAR();
  if (t < 64) st8_sc1(p.ws, WS_F1FLAG + t * 512 + w * 8, tag);
  STAMP(1);
  float* rst = (float*)(smem + SM_OFF + 2624);
  if (t < 576) st_sc1(rs, oslot + t * 16, u32x4{0u, ~tag32, 0u, ~tag32});
  DRAIN();
  BAR();
  if (t == 0) st8_sc1(p.ws, WS_F1FLAGC + w * 8, tag);
  if (wave < 8) {
    while (__builtin_amdgcn_s_memrealtime() - t_start < (u64)LATE_DMA) __builtin_amdgcn_s_sleep(4);
    wo_dma(p.woB + (size_t)col0 * 576, lds0 + WO_OFF, wave, lane);
  } else {
    while (__builtin_amdgcn_s_memrealtime() - t_start < (u64)LATE_UV) __builtin_amdgcn_s_sleep(4);
#pragma unroll
    for (int it = 0; it < 8; ++it) {
      const int n = (wave - 8) * 8 + it;
      const u32 row = n * 8 + (lane >> 3);
      glds16(p.wuv + (size_t)row * 4096 + col0 + (((lane & 7) ^ wsw(row)) << 3), lds0 + UV_OFF + n * 1024);
    }
  }
  STAMP(2);
  {
    const int bb = wave >> 3, sg = wave & 7;
    if (wave == 0) poll_flags(p.ws, WS_F1FLAGC + lane * 8, true, tag);
    poll_flags(p.ws, WS_F2FLAG2 + w * 512 + (2 * (4 * sg + (lane & 3)) + bb) * 8, lane < 4, tag);
    u32x4 pv[4];
#pragma unroll
    for (int u = 0; u < 4; ++u) pv[u] = ld_sc1(rs, WS_OLAT + (u32)(((h * 2 + bb) * 32 + 4 * sg + u) * 1024 + lane * 16));
    const u64 ml = ld8_sc1(p.ws, WS_ML + (u32)(((h * 2 + bb) * 32 + 4 * sg + (lane & 3)) * 8));
    const float ms = __uint_as_float((u32)ml), ls = __uint_as_float((u32)(ml >> 32));
    float M = ms;
    M = fmaxf(M, dpp<XOR1>(M)); M = fmaxf(M, dpp<XOR2>(M));
    const float wl = ls * ex2(ms - M);
    float L = wl;
    L += dpp<XOR1>(L); L += dpp<XOR2>(L);
    if (lane == 0) *(f32x2*)(rst + wave * 2) = f32x2{M, L};
    float a[8];
#pragma unroll
    for (int e = 0; e < 8; ++e) a[e] = 0.f;
#pragma unroll
    for (int u = 0; u < 4; ++u) {
      const float wu = u == 0 ? dpp<0x00>(wl) : u == 1 ? dpp<0x55>(wl) : u == 2 ? dpp<0xAA>(wl) : dpp<0xFF>(wl);
#pragma unroll
      for (int e = 0; e < 8; ++e) a[e] = fmaf(wu, (e & 1) ? bfhi(pv[u][e >> 1]) : bflo(pv[u][e >> 1]), a[e]);
    }
    *(u32x4*)(smem + AI_OFF + (lane >> 4) * 4096 + off_b(wave, lane & 15)) =
        u32x4{pk2(a[0], a[1]), pk2(a[2], a[3]), pk2(a[4], a[5]), pk2(a[6], a[7])};
    DRAIN();
  }
  STAMP(3);
  BAR();
  {
    const int nt = wave & 3, kq = wave >> 2;
    u32x4 af[4], bf[4];
#pragma unroll
    for (int u = 0; u < 4; ++u) {
      af[u] = *(const u32x4*)(smem + AI_OFF + kq * 4096 + off_b(lane & 15, 4 * u + (lane >> 4)));
      bf[u] = wfrag(smem, UV_OFF, 128, 4 * kq + u, nt, lane);
    }
    const float* rb = rst + (lane >> 5) * 16;
    float M = rb[0];
#pragma unroll
    for (int u = 1; u < 8; ++u) M = fmaxf(M, rb[2 * u]);
    float L = 0.f;
#pragma unroll
    for (int u = 0; u < 8; ++u) L = fmaf(ex2(rb[2 * u] - M), rb[2 * u + 1], L);
    const float* rr = rst + (lane >> 4) * 8;
    const float li = 1.f / L;
    f32x4 acc = {0.f, 0.f, 0.f, 0.f};
#pragma unroll
    for (int u = 0; u < 4; ++u) acc = mfma16(af[u], bf[u], acc);
    float sv = 0.f;
#pragma unroll
    for (int u = 0; u < 4; ++u) sv = fmaf(acc[u], ex2(rr[2 * u] - M), sv);
    sv = x16_sum(sv) * li;
    if ((lane & 16) == 0) part[(kq * 2 + (lane >> 5)) * 64 + nt * 16 + (lane & 15)] = sv;
  }
  STAMP(12);
  BAR();
  if (t < 128) z_store(smem, ZIMG, t >> 6, t & 63, (part[t] + part[128 + t]) + (part[256 + t] + part[384 + t]));
  BAR();
  STAMP(13);
  oproj_mfma(smem, WO_OFF, ZIMG, rs, oslot, tag32, wave, lane);
  STAMP(4);
  if (t < 576) {
    const int pp = t / 9, i = t - pp * 9;
    const u32 off = WS_OG + (u32)(pp * 9216 + (9 * w + i) * 16);
    const f32x2 vb = spin_pair(rs, off + 64 * 9216, tag32);
    STAMP(5);
    const f32x2 va = spin_pair(rs, off, tag32);
    const float g0 = gsh[0], g1 = gsh[1];
    *(f32x2*)(fin + 2 * t) = f32x2{g0 * va[0] + (1.f - g0) * vb[0], g1 * va[1] + (1.f - g1) * vb[1]};
  }
  STAMP(6);
  BAR();
  if (t < 192) {
    const int o = t >> 3, k = t & 7;
    float sum = 0.f;
    if (o < 18) {
#pragma unroll
      for (int u = 0; u < 8; ++u) sum += fin[(k * 8 + u) * 18 + o];
    }
    sum += dpp<XOR1>(sum); sum += dpp<XOR2>(sum); sum += dpp<XOR7>(sum);
    if (k == 0 && o < 18) p.out[(o & 1) * 576 + 9 * w + (o >> 1)] = bf1(sum);
    if (w == 0 && t == 0) st8_sc1(p.ws, WS_EPOCH, tag);
  }
  STAMP(7);
}

__global__ void __launch_bounds__(1024, 4) fused_kernel(Params p) {
  __shared__ __attribute__((aligned(16))) unsigned char smem[LDS_BYTES];
  const u64 epoch = *(const __attribute__((address_space(4))) u64*)(p.ws + WS_EPOCH);
  STAMP(0);
  const u64 tag = mix64(epoch);
  const __amdgpu_buffer_rsrc_t rs = __builtin_amdgcn_make_buffer_rsrc(p.ws, 0, WS_END, 0x00020000);
  const int w = blockIdx.x;
  if (w < 64) role_F1(p, smem, rs, tag, w);
  else if (w < 128) role_F2(p, smem, rs, tag, w - 64);
  else {
    role_A(p, smem, rs, tag, w - 128);
  }
}

__global__ void lead_kernel(unsigned char* ws) {
  ((u64*)(ws + WS_DBG))[2 * 256 * 16 * 16] = __builtin_amdgcn_s_memrealtime();
}
__global__ void spin_kernel(unsigned char* ws) {
  const u64 tin = __builtin_amdgcn_s_memrealtime();
  const u64* dbg = (const u64*)(ws + WS_DBG);
  __shared__ u64 smn[256], smx[256];
  const int w = threadIdx.x;
  u64 mn = ~0ull;
  for (int v = 0; v < 16; ++v) { const u64 x = dbg[(w * 16 + v) * 16]; mn = x < mn ? x : mn; }
  smn[w] = mn;
  __syncthreads();
  u64 t0 = ~0ull;
  for (int i = 0; i < 256; ++i) t0 = smn[i] < t0 ? smn[i] : t0;
  u64 mx = 0;
  if (w < 64)
    for (int v = 0; v < 16; ++v) { const u64 x = dbg[(w * 16 + v) * 16 + 7]; if (x >= t0 && x < t0 + 100000 && x - t0 > mx) mx = x - t0; }
  smx[w] = mx;
  __syncthreads();
  u64 D = 0;
  for (int i = 0; i < 256; ++i) D = smx[i] > D ? smx[i] : D;
  if (0 == 0) D = 0;
  if (0 == 1) D = tin - (t0 + D);
  if (0 == 2) D = t0 - dbg[2 * 256 * 16 * 16];
  if (D > 5000) D = 5000;
  const u64 s0 = __builtin_amdgcn_s_memrealtime();
  while (__builtin_amdgcn_s_memrealtime() - s0 < D * 10) __builtin_amdgcn_s_sleep(8);
}
}

extern "C" void kernel_launch(void* const* d_in, const int* in_sizes, int n_in,
                              void* d_out, int out_size, void* d_ws, size_t ws_size,
                              hipStream_t stream) {
  Params p;
  p.x = (const u16*)d_in[0];
  p.K = (const u16*)d_in[1];
  p.V = (const u16*)d_in[2];
  p.wqA = (const u16*)d_in[3];
  p.woA = (const u16*)d_in[4];
  p.ckv = (const u16*)d_in[6];
  p.wqB = (const u16*)d_in[7];
  p.wuk = (const u16*)d_in[8];
  p.wuv = (const u16*)d_in[9];
  p.woB = (const u16*)d_in[10];
  p.gw = (const u16*)d_in[11];
  p.out = (u16*)d_out;
  p.ws = (unsigned char*)d_ws;
  hipLaunchKernelGGL(lead_kernel, dim3(1), dim3(1), 0, stream, p.ws);
  hipLaunchKernelGGL(fused_kernel, dim3(256), dim3(1024), 0, stream, p);
  hipLaunchKernelGGL(spin_kernel, dim3(1), dim3(256), 0, stream, p.ws);
}
```

```cpp
#include <hip/hip_runtime.h>
#include <hip/hip_bf16.h>
#include <stdint.h>

namespace {

typedef unsigned short u16;
typedef unsigned int u32;
typedef unsigned long long u64;
typedef u32 u32x4 __attribute__((ext_vector_type(4)));
typedef u32 u32x2 __attribute__((ext_vector_type(2)));
typedef float f32x4 __attribute__((ext_vector_type(4)));
typedef float f32x2 __attribute__((ext_vector_type(2)));
typedef float f32x16 __attribute__((ext_vector_type(16)));
typedef short s16x4 __attribute__((ext_vector_type(4)));
typedef __bf16 bf16x2 __attribute__((ext_vector_type(2)));
typedef __bf16 bf16x8 __attribute__((ext_vector_type(8)));
#define GAS __attribute__((address_space(1)))
#define LAS __attribute__((address_space(3)))
#define RLX_AGENT __ATOMIC_RELAXED, __HIP_MEMORY_SCOPE_AGENT
#define DI __device__ __forceinline__

constexpr u32 WS_EPOCH = 0;
constexpr u32 WS_AFLAG = 3072;
constexpr u32 WS_F1FLAGC = 4096;
constexpr u32 WS_QA = 8192;
constexpr u32 WS_QABS = 8192 + 65536;
constexpr u32 WS_ML = WS_QABS + 131072;
constexpr u32 WS_OLAT = WS_ML + 16384;
constexpr u32 WS_AG = WS_OLAT + 2097152;
constexpr u32 WS_OG = WS_AG + 64 * 8 * 66 * 8;
constexpr u32 WS_F1FLAG = WS_OG + 2 * 64 * 576 * 16;
constexpr u32 WS_F2FLAG2 = WS_F1FLAG + 32768;
constexpr u32 WS_END = WS_F2FLAG2 + 32768;

constexpr int LDS_BYTES = 163840;
constexpr u32 QOFF = 131072;
constexpr float C2 = 0.08838834764831845f * 1.4426950408889634f;
constexpr u32 SPIN_CAP = 1u << 18;
#define A_SLOW 3
#define A_SLOWD 1
#define DMA_EARLY 4
#define UK_DELAY 40
#define LATE_DMA 1200
#define POLL_SLEEP 4
#define F2_QDELAY 0
#define LATE_UV 1200
#define A_DEPTH 4
#define WAITV_(n) asm volatile("s_waitcnt vmcnt(" #n ")" ::: "memory")
#define WAITV(n) WAITV_(n)
#define AD1_2 1
#define AD1_3 2
#define AD1_4 3
#define AD1_5 4
#define AD1_6 5
#define AD1__(d) AD1_##d
#define AD1_(d) AD1__(d)

#define BAR() asm volatile("s_waitcnt lgkmcnt(0)\n\ts_barrier" ::: "memory")
#define DRAIN() asm volatile("s_waitcnt vmcnt(0)" ::: "memory")
#define SCHED() __builtin_amdgcn_sched_barrier(0)

struct Params {
  const u16 *x, *K, *V, *wqA, *woA, *ckv, *wqB, *wuk, *wuv, *woB, *gw;
  u16* out;
  unsigned char* ws;
};

DI float bflo(u32 v) { return __uint_as_float(v << 16); }
DI float bfhi(u32 v) { return __uint_as_float(v & 0xffff0000u); }
DI u32 pk2(float lo, float hi) {
  bf16x2 r; r[0] = (__bf16)lo; r[1] = (__bf16)hi; return __builtin_bit_cast(u32, r);
}
DI u16 bf1(float v) { const __bf16 r = (__bf16)v; return __builtin_bit_cast(u16, r); }
DI float dot2(u32 a, u32 b, float acc) {
  return __builtin_amdgcn_fdot2_f32_bf16(__builtin_bit_cast(bf16x2, a), __builtin_bit_cast(bf16x2, b), acc, false);
}
DI float dot8(u32x4 a, u32x4 b) {
  float d = dot2(a[0], b[0], 0.f); d = dot2(a[1], b[1], d); d = dot2(a[2], b[2], d); return dot2(a[3], b[3], d);
}
template <int CTRL> DI float dpp(float x) {
  return __builtin_bit_cast(float, __builtin_amdgcn_mov_dpp(__builtin_bit_cast(int, x), CTRL, 0xf, 0xf, true));
}
constexpr int XOR1 = 0xB1, XOR2 = 0x4E, XOR7 = 0x141, ROR8 = 0x128, ROR4 = 0x124;
DI float tr32(float a, float b) {
  auto s = __builtin_amdgcn_permlane32_swap(__float_as_uint(a), __float_as_uint(b), false, false);
  return __uint_as_float(s[0]) + __uint_as_float(s[1]);
}
DI float tr16(float a, float b) {
  auto s = __builtin_amdgcn_permlane16_swap(__float_as_uint(a), __float_as_uint(b), false, false);
  return __uint_as_float(s[0]) + __uint_as_float(s[1]);
}
DI float x16_max(float x) {
  auto s = __builtin_amdgcn_permlane16_swap(__float_as_uint(x), __float_as_uint(x), false, false);
  return fmaxf(__uint_as_float(s[0]), __uint_as_float(s[1]));
}
DI float x32_max(float x) {
  auto s = __builtin_amdgcn_permlane32_swap(__float_as_uint(x), __float_as_uint(x), false, false);
  return fmaxf(__uint_as_float(s[0]), __uint_as_float(s[1]));
}
DI float x16_sum(float x) { return tr16(x, x); }
DI float x32_sum(float x) { return tr32(x, x); }
DI float ex2(float x) { return __builtin_amdgcn_exp2f(x); }

DI u32x4 ldg16(const u16* p) { return *(const GAS u32x4*)p; }
DI u32x4 ldg16nt(const u16* p) { return __builtin_nontemporal_load((const GAS u32x4*)p); }
DI u32x4 ld_sc1(__amdgpu_buffer_rsrc_t rs, u32 off) { return __builtin_amdgcn_raw_buffer_load_b128(rs, off, 0, 16); }
DI void st_sc1(__amdgpu_buffer_rsrc_t rs, u32 off, u32x4 v) { __builtin_amdgcn_raw_buffer_store_b128(v, rs, off, 0, 16); }
DI u64 ld8_sc1(unsigned char* ws, u32 off) { return __hip_atomic_load((GAS u64*)(ws + off), RLX_AGENT); }
DI void st8_sc1(unsigned char* ws, u32 off, u64 v) { __hip_atomic_store((GAS u64*)(ws + off), v, RLX_AGENT); }

DI u64 mix64(u64 e) {
  u64 z = e + 0x9E3779B97F4A7C15ull;
  z = (z ^ (z >> 30)) * 0xBF58476D1CE4E5B9ull;
  z = (z ^ (z >> 27)) * 0x94D049BB133111EBull;
  return z ^ (z >> 31);
}
DI u64 tag_of(u64 e) {
  u32 lo = (u32)e, hi = (u32)(e >> 32);
  asm volatile("" : "+v"(lo), "+v"(hi) :: "memory");
  lo = __builtin_amdgcn_readfirstlane(lo); hi = __builtin_amdgcn_readfirstlane(hi);
  return mix64(((u64)hi << 32) | lo);
}
DI void poll_flags(unsigned char* ws, u32 off, bool active, u64 tag) {
  for (u32 spins = 0; spins < SPIN_CAP; ++spins) {
    bool ok = true;
    if (active) ok = (ld8_sc1(ws, off) == tag);
    if (__all(ok)) break;
    __builtin_amdgcn_s_sleep(POLL_SLEEP);
  }
  __builtin_amdgcn_fence(__ATOMIC_ACQUIRE, "wavefront");
}
DI f32x2 spin_pair(__amdgpu_buffer_rsrc_t rs, u32 off, u32 tag32) {
  u32x4 v;
  for (u32 spins = 0; spins < SPIN_CAP; ++spins) {
    asm volatile("" ::: "memory");
    v = ld_sc1(rs, off);
    bool ok = (v[1] == tag32) & (v[3] == tag32);
    if (__all(ok)) break;
  }
  f32x2 r; r[0] = __uint_as_float(v[0]); r[1] = __uint_as_float(v[2]); return r;
}
DI void glds16(const void* gsrc, u32 lds_dst) {
  u32 keep;
  asm volatile("s_mov_b32 %0, m0\n\ts_mov_b32 m0, %2\n\ts_nop 0\n\tglobal_load_lds_dwordx4 %1, off nt\n\ts_mov_b32 m0, %0"
               : "=&s"(keep) : "v"(gsrc), "s"(lds_dst) : "memory");
}
DI u32 off_b(u32 row, u32 ch) { return 256u * row + 16u * (ch ^ (((row & 3) << 2) | ((row >> 2) & 3))); }
DI u32 wsw(u32 row) { return (((row >> 1) & 1) | (((row >> 3) & 1) << 1)) << 1; }
DI f32x4 mfma16(u32x4 a, u32x4 b, f32x4 c) {
  return __builtin_amdgcn_mfma_f32_16x16x32_bf16(__builtin_bit_cast(bf16x8, a), __builtin_bit_cast(bf16x8, b), c, 0, 0, 0);
}
DI f32x16 mfma32(u32x4 a, u32x4 b, f32x16 c) {
  return __builtin_amdgcn_mfma_f32_32x32x16_bf16(__builtin_bit_cast(bf16x8, a), __builtin_bit_cast(bf16x8, b), c, 0, 0, 0);
}
DI u32x4 tr_pair(const unsigned char* smem, u32 a0, u32 a1) {
  const s16x4 lo = __builtin_amdgcn_ds_read_tr16_b64_v4i16((LAS s16x4*)(smem + a0));
  const s16x4 hi = __builtin_amdgcn_ds_read_tr16_b64_v4i16((LAS s16x4*)(smem + a1));
  return __builtin_bit_cast(u32x4, __builtin_shufflevector(lo, hi, 0, 1, 2, 3, 4, 5, 6, 7));
}
DI u32x4 wfrag(const unsigned char* smem, u32 img, u32 stride, u32 ks, u32 ct, int lane) {
  const u32 g = lane >> 4, q = (lane & 15) >> 2, pp = lane & 3;
  const u32 r0 = 32 * ks + 8 * g + q, r1 = r0 + 4;
  const u32 ch = 2 * ct + (pp >> 1);
  return tr_pair(smem, img + r0 * stride + ((ch ^ wsw(r0)) << 4) + 8 * (pp & 1),
                       img + r1 * stride + ((ch ^ wsw(r1)) << 4) + 8 * (pp & 1));
}

#define STAMP(i) do {} while (0)

struct Gemv1 { u32x4 w[8]; u32x4 x0, x1; };
DI void gemv1_issue(Gemv1& g, const u16* W, const u16* x, int t) {
  const int rg = t >> 3, ch = t & 7;
  const u16* wp = W + (size_t)(8 * rg) * 4096 + 8 * ch;
  g.x0 = ldg16(x + 8 * rg);
  g.x1 = ldg16(x + 576 + 8 * rg);
#pragma unroll
  for (int k = 0; k < 8; ++k) g.w[k] = ldg16nt(wp + (size_t)k * 4096);
}
DI void gemv1_finish(Gemv1& g, float* red  , int wave, int lane) {
  float a0[8], a1[8];
#pragma unroll
  for (int e = 0; e < 8; ++e) { a0[e] = 0.f; a1[e] = 0.f; }
#pragma unroll
  for (int k = 0; k < 8; ++k) {
    const float xa = (k & 1) ? bfhi(g.x0[k >> 1]) : bflo(g.x0[k >> 1]);
    const float xb = (k & 1) ? bfhi(g.x1[k >> 1]) : bflo(g.x1[k >> 1]);
#pragma unroll
    for (int e = 0; e < 8; ++e) {
      const float wf = (e & 1) ? bfhi(g.w[k][e >> 1]) : bflo(g.w[k][e >> 1]);
      a0[e] = fmaf(xa, wf, a0[e]); a1[e] = fmaf(xb, wf, a1[e]);
    }
  }
  float v[8], u[4], wv[2];
#pragma unroll
  for (int e = 0; e < 8; ++e) v[e] = tr32(a0[e], a1[e]);
#pragma unroll
  for (int e = 0; e < 4; ++e) u[e] = tr16(v[e], v[e + 4]);
#pragma unroll
  for (int e = 0; e < 2; ++e) {
    const float t1 = u[e] + dpp<ROR8>(u[e]), t2 = u[e + 2] + dpp<ROR8>(u[e + 2]);
    wv[e] = (lane & 8) ? t2 : t1;
  }
  *(f32x2*)(red + wave * 128 + (lane >> 5) * 64 + (lane & 7) * 8 + ((lane >> 3) & 1) * 2 + ((lane >> 4) & 1) * 4) = f32x2{wv[0], wv[1]};
}
DI u32x4 gemv1_sum(const float* red, int t) {
  f32x4 s0 = {0.f, 0.f, 0.f, 0.f}, s1 = {0.f, 0.f, 0.f, 0.f};
  const float* r = red + (t >> 3) * 64 + (t & 7) * 8;
#pragma unroll
  for (int v = 0; v < 9; ++v) { s0 += *(const f32x4*)(r + v * 128); s1 += *(const f32x4*)(r + v * 128 + 4); }
  return u32x4{pk2(s0[0], s0[1]), pk2(s0[2], s0[3]), pk2(s1[0], s1[1]), pk2(s1[2], s1[3])};
}
DI void wo_dma(const u16* W, u32 lds_img, int wv, int lane) {
#pragma unroll 3
  for (int it = 0; it < 9; ++it) {
    const int n = wv * 9 + it;
    const u32 P = n * 1024 + lane * 16, row = P / 1152, cpos = (P - row * 1152) >> 4;
    glds16(W + (size_t)row * 576 + ((cpos ^ wsw(row)) << 3), lds_img + n * 1024);
  }
}
DI void oproj_mfma(const unsigned char* smem, u32 img, u32 zimg, __amdgpu_buffer_rsrc_t rs, u32 slot, u32 tag32, int wave, int lane) {
  u32x4 a[2];
#pragma unroll
  for (int ks = 0; ks < 2; ++ks) {
    a[ks] = u32x4{0u, 0u, 0u, 0u};
    if ((lane & 15) < 4) a[ks] = *(const u32x4*)(smem + zimg + (lane & 15) * 128 + (32 * ks + 8 * (lane >> 4)) * 2);
  }
  const int nt = wave < 4 ? 3 : 2;
  u32x4 bfr[3][2];
#pragma unroll
  for (int i = 0; i < 3; ++i)
    if (i < nt) {
#pragma unroll
      for (int ks = 0; ks < 2; ++ks) bfr[i][ks] = wfrag(smem, img, 1152, ks, wave + 16 * i, lane);
    }
#pragma unroll
  for (int i = 0; i < 3; ++i)
    if (i < nt) {
      f32x4 acc = {0.f, 0.f, 0.f, 0.f};
      acc = mfma16(a[0], bfr[i][0], acc);
      acc = mfma16(a[1], bfr[i][1], acc);
      if (lane < 16)
        st_sc1(rs, slot + (u32)(((wave + 16 * i) * 16 + lane) * 16),
               u32x4{__float_as_uint(acc[0] + acc[2]), tag32, __float_as_uint(acc[1] + acc[3]), tag32});
    }
}
DI void z_store(unsigned char* smem, u32 zimg, int b, int d, float z) {
  const u16 hi = bf1(z);
  const u16 lo = bf1(z - __uint_as_float((u32)hi << 16));
  *(u16*)(smem + zimg + b * 128 + d * 2) = hi;
  *(u16*)(smem + zimg + (2 + b) * 128 + d * 2) = lo;
}

#define TR_STEP(HALF, CTRL, BIT)                                                          \
  _Pragma("unroll") for (int h_ = 0; h_ < (HALF); ++h_) {                                 \
    const float lo_ = x[h_], hi_ = x[h_ + (HALF)];                                        \
    const float t1_ = lo_ + dpp<CTRL>(lo_), t2_ = hi_ + dpp<CTRL>(hi_);                   \
    x[h_] = (c & (BIT)) ? t2_ : t1_;                                                      \
  }

constexpr int NLA = 16;
constexpr int NSP = 4;
DI void ag_clear(__amdgpu_buffer_rsrc_t rs, int hkv, int bs, u32 ntag, int lane) {
#pragma unroll
  for (int k = 0; k < 3; ++k) {
    const int n = lane + 64 * k;
    if (n < 132) {
      const int seg = n / 33, pos = n - seg * 33;
      st_sc1(rs, WS_AG + (u32)((((hkv * 4 + seg) * 8 + bs) * 66 + 2 * pos) * 8), u32x4{0u, ntag, 0u, ntag});
    }
  }
}
DI void q_fetch(__amdgpu_buffer_rsrc_t rs, unsigned char* qs, int b, int hkv, u64 tag, int lane) {
  const u32 thi = (u32)(tag >> 32), tlo = (u32)tag & 0xffff0000u;
  const u32 off = WS_QA + (u32)((b * 4096 + hkv * 256 + lane * 4) * 8);
  u32x4 g0, g1;
  for (u32 spins = 0; spins < SPIN_CAP; ++spins) {
    asm volatile("" ::: "memory");
    g0 = ld_sc1(rs, off); g1 = ld_sc1(rs, off + 16);
    const bool ok = (g0[1] == thi) & (g0[3] == thi) & (g1[1] == thi) & (g1[3] == thi) &
                    ((g0[0] & 0xffff0000u) == tlo) & ((g0[2] & 0xffff0000u) == tlo) &
                    ((g1[0] & 0xffff0000u) == tlo) & ((g1[2] & 0xffff0000u) == tlo);
    if (__all(ok)) break;
    __builtin_amdgcn_s_sleep(1);
  }
  *(u32x2*)(qs + lane * 8) = u32x2{(g0[0] & 0xffffu) | (g0[2] << 16), (g1[0] & 0xffffu) | (g1[2] << 16)};
}
template <int NL>
DI void gqa_wave(u32x4 (&kv)[NL], const u16* Vp, const unsigned char* qs, float* pw, float* mrg, float* mls, int wave, int lane) {
  constexpr int NG = (NL + 7) / 8, NLP = NL <= 4 ? 4 : 8 * NG;
  const int r = lane >> 4, c = lane & 15;
  const u32x4 q0 = *(const u32x4*)(qs + c * 16), q1 = *(const u32x4*)(qs + 256 + c * 16);
  float sc[NG];
#pragma unroll
  for (int G = 0; G < NG; ++G) {
    float x[16];
#pragma unroll
    for (int ii = 0; ii < 8; ++ii) {
      const int i = 8 * G + ii;
      if (i < NL) {
        x[2 * ii] = dot8(kv[i], q0);
        x[2 * ii + 1] = dot8(kv[i], q1);
        SCHED();
        kv[i] = ldg16nt(Vp + i * 512);
        SCHED();
      } else { x[2 * ii] = 0.f; x[2 * ii + 1] = 0.f; }
    }
    TR_STEP(8, ROR8, 8) TR_STEP(4, XOR7, 4) TR_STEP(2, XOR2, 2) TR_STEP(1, XOR1, 1)
    sc[G] = (8 * G + (c >> 1) < NL) ? x[0] * C2 : -INFINITY;
  }
  float m = sc[0];
#pragma unroll
  for (int G = 1; G < NG; ++G) m = fmaxf(m, sc[G]);
  m = fmaxf(m, dpp<XOR2>(m)); m = fmaxf(m, dpp<ROR4>(m)); m = fmaxf(m, dpp<ROR8>(m));
  m = x32_max(x16_max(m));
  float l = 0.f;
#pragma unroll
  for (int G = 0; G < NG; ++G) {
    const float pe = ex2(sc[G] - m);
    l += pe;
    if (16 * G + c < 2 * NLP) pw[r * 2 * NLP + 16 * G + c] = pe;
  }
  l += dpp<XOR2>(l); l += dpp<ROR4>(l); l += dpp<ROR8>(l);
  l = x32_sum(x16_sum(l));
  float a0[8], a1[8];
#pragma unroll
  for (int e = 0; e < 8; ++e) { a0[e] = 0.f; a1[e] = 0.f; }
#pragma unroll
  for (int i2 = 0; i2 < (NL + 1) / 2; ++i2) {
    const f32x4 pp = *(const f32x4*)(pw + r * 2 * NLP + i2 * 4);
#pragma unroll
    for (int ii = 0; ii < 2; ++ii) {
      const int i = 2 * i2 + ii;
      if (i < NL) {
        const float pa = pp[2 * ii], pb = pp[2 * ii + 1];
#pragma unroll
        for (int e = 0; e < 8; ++e) {
          const float vf = (e & 1) ? bfhi(kv[i][e >> 1]) : bflo(kv[i][e >> 1]);
          a0[e] = fmaf(pa, vf, a0[e]); a1[e] = fmaf(pb, vf, a1[e]);
        }
      }
    }
  }
  float v[8];
#pragma unroll
  for (int e = 0; e < 8; ++e) v[e] = tr32(a0[e], a1[e]);
  f32x4 u;
#pragma unroll
  for (int e = 0; e < 4; ++e) u[e] = tr16(v[e], v[e + 4]);
  *(f32x4*)(mrg + wave * 256 + (lane >> 5) * 128 + c * 8 + ((lane >> 4) & 1) * 4) = u;
  if (lane < 2) *(f32x2*)(mls + wave * 4 + lane * 2) = f32x2{m, l};
}
DI void gqa_merge(unsigned char* ws, const float* mrg, const float* mls, int hkv, int bs, u32 tag32, int t) {
  const int g = t >> 7, d = t & 127;
  float mv[16], M = -INFINITY;
#pragma unroll
  for (int v = 0; v < 16; ++v) { mv[v] = mls[v * 4 + g * 2]; M = fmaxf(M, mv[v]); }
  float L = 0.f, A = 0.f;
#pragma unroll
  for (int v = 0; v < 16; ++v) {
    const float w = ex2(mv[v] - M);
    L = fmaf(w, mls[v * 4 + g * 2 + 1], L);
    A = fmaf(w, mrg[v * 256 + t], A);
  }
  const u32 slot = WS_AG + (u32)((((hkv * 2 + g) * 2 + (d >> 6)) * 8 + bs) * 66 * 8);
  st8_sc1(ws, slot + (d & 63) * 8, ((u64)tag32 << 32) | __float_as_uint(A));
  if ((d & 63) < 2) st8_sc1(ws, slot + (64 + (d & 1)) * 8, ((u64)tag32 << 32) | __float_as_uint((d & 1) ? L : M));
}

DI void role_A(const Params& p, unsigned char* smem, __amdgpu_buffer_rsrc_t rs, u64 epoch, int a) {
  const int t = threadIdx.x, lane = t & 63, wave = __builtin_amdgcn_readfirstlane(t >> 6);
  const int hkv = a & 15, b = (a >> 4) & 1, s = a >> 5;
  const size_t base = ((size_t)(b * 16 + hkv) * 4096 + s * (64 * NLA) + wave * (4 * NLA) + (lane >> 4)) * 128 + (lane & 15) * 8;
  const u16* Kp = p.K + base;
  unsigned char* qs = smem;
  float* pw = (float*)(smem + 1024) + wave * 128;
  float* mrg = (float*)(smem + 1024 + 8192);
  float* mls = mrg + 16 * 256;
  u32x4 kv[NLA];
  u64 tag;
  if (wave == 0) {
    tag = tag_of(epoch);
    ag_clear(rs, hkv, b * 4 + s, ~(u32)tag, lane);
    DRAIN();
    if (lane == 0) st8_sc1(p.ws, WS_AFLAG + a * 8, tag);
    q_fetch(rs, qs, b, hkv, tag, lane);
    STAMP(1);
    BAR();
#pragma unroll
    for (int i = 0; i < NLA; ++i) kv[i] = ldg16nt(Kp + i * 512);
  } else {
#pragma unroll
    for (int i = 0; i < NLA; ++i) {
      kv[i] = ldg16nt(Kp + i * 512);
      if (i < A_SLOW) {
        if (A_SLOWD == 1) asm volatile("s_waitcnt vmcnt(0)" ::: "memory");
        else if (i >= 1) asm volatile("s_waitcnt vmcnt(1)" ::: "memory");
      } else if (i >= A_DEPTH - 1 && i < NLA - 1) WAITV(AD1_(A_DEPTH));
    }
    tag = tag_of(epoch);
    STAMP(1);
    BAR();
  }
  STAMP(2);
  gqa_wave<NLA>(kv, p.V + base, qs, pw, mrg, mls, wave, lane);
  STAMP(3);
  BAR();
  if (t < 256) gqa_merge(p.ws, mrg, mls, hkv, b * 4 + s, (u32)tag, t);
  STAMP(4);
}

DI void ckv_dma(const Params& p, u32 lds0, int b, int s, int w4, int lane, int it) {
  const int n = it * 4 + w4;
  const u32 row = 4 * (n >> 2) + (lane >> 4), cb = n & 3;
  const u32 ch = (lane & 15) ^ (((row & 3) << 2) | ((row >> 2) & 3));
  glds16(p.ckv + ((size_t)(b * 4096 + s * 128 + row) * 512 + cb * 128 + ch * 8), lds0 + cb * 32768 + (n >> 2) * 1024);
}
DI void role_F2(const Params& p, unsigned char* smem, __amdgpu_buffer_rsrc_t rs, u64 epoch, int j) {
  const int t = threadIdx.x, lane = t & 63, wave = __builtin_amdgcn_readfirstlane(t >> 6);
  const int b = j & 1, s = j >> 1;
  float* red = (float*)(smem + QOFF);
  const u32 lds0 = (u32)(size_t)smem;
  const u32 oslot = WS_OG + (u32)(j * 9216);
  if (wave < 9) {
    Gemv1 g;
    gemv1_issue(g, p.wqA + 64 * j, p.x, t);
    gemv1_finish(g, red, wave, lane);
    STAMP(9);
  } else if (wave >= 12) {
    for (int it = 0; it < DMA_EARLY; ++it) {
      ckv_dma(p, lds0, b, s, wave - 12, lane, it);
      if (F2_QDELAY) WAITV(7); else WAITV(1);
    }
  }
  const u64 tag = tag_of(epoch);
  const u32 tag32 = (u32)tag;
  STAMP(1);
  BAR();
  if (wave == 0) {
    f32x2 sq = {0.f, 0.f};
#pragma unroll
    for (int v = 0; v < 9; ++v) sq += *(const f32x2*)(red + v * 128 + 2 * lane);
    const u32 thi = (u32)(tag >> 32), tlo = (u32)tag & 0xffff0000u;
    st_sc1(rs, WS_QA + (u32)(((lane >> 5) * 4096 + 64 * j + (lane & 31) * 2) * 8),
           u32x4{tlo | bf1(sq[0]), thi, tlo | bf1(sq[1]), thi});
    STAMP(2);
  }
  BAR();
  if (wave < 9) {
    st_sc1(rs, oslot + t * 16, u32x4{0u, ~tag32, 0u, ~tag32});
  } else if (wave == 9) {
    poll_flags(p.ws, WS_AFLAG + ((j >> 2) + 16 * (lane & 7)) * 8, lane < 8, tag);
  } else if (wave >= 12) {
#pragma unroll 4
    for (int it = DMA_EARLY; it < 32; ++it) { ckv_dma(p, lds0, b, s, wave - 12, lane, it); WAITV(7); }
  }
  {
    poll_flags(p.ws, WS_F1FLAG + j * 512 + (wave + 16 * (lane & 3)) * 8, lane < 4, tag);
    u32x4 v0[2], v1[2];
#pragma unroll
    for (int u = 0; u < 2; ++u) {
      const int n = t + 1024 * u;
      v0[u] = ld_sc1(rs, WS_QABS + (u32)((b * 2 + 0) * 32768 + n * 16));
      v1[u] = ld_sc1(rs, WS_QABS + (u32)((b * 2 + 1) * 32768 + n * 16));
    }
#pragma unroll
    for (int u = 0; u < 2; ++u) {
      const int n = t + 1024 * u;
      u32x4 o;
#pragma unroll
      for (int e = 0; e < 4; ++e) o[e] = pk2(bflo(v0[u][e]) + bflo(v1[u][e]), bfhi(v0[u][e]) + bfhi(v1[u][e]));
      const u32 h = n >> 6, c8 = n & 63;
      *(u32x4*)(smem + QOFF + (c8 >> 4) * 8192 + off_b(h, c8 & 15)) = o;
    }
    DRAIN();
  }
  STAMP(3);
  BAR();
  const int l31 = lane & 31, hf = lane >> 5;
  float* stat = (float*)(smem + QOFF + 8192);
  f32x16 sacc;
  float Mh = 0.f;
  if (wave < 4) {
#pragma unroll
    for (int i = 0; i < 16; ++i) sacc[i] = 0.f;
    u32x4 av[2][4], bv[2][4];
#define S_LOAD(BT)                                                                              \
  _Pragma("unroll") for (int u = 0; u < 4; ++u) {                                               \
    const u32 ks = 4 * (BT) + u, cb = ks >> 3, ss = ks & 7;                                      \
    av[(BT) & 1][u] = *(const u32x4*)(smem + cb * 32768 + off_b(32 * wave + l31, 2 * ss + hf));  \
    bv[(BT) & 1][u] = *(const u32x4*)(smem + QOFF + cb * 8192 + off_b(l31, 2 * ss + hf));        \
  }
    S_LOAD(0)
#pragma unroll
    for (int bt = 0; bt < 8; ++bt) {
      if (bt < 7) { S_LOAD(bt + 1) }
      SCHED();
#pragma unroll
      for (int u = 0; u < 4; ++u) sacc = mfma32(av[bt & 1][u], bv[bt & 1][u], sacc);
      SCHED();
    }
    float mx = -INFINITY;
#pragma unroll
    for (int i = 0; i < 16; ++i) { sacc[i] *= C2; mx = fmaxf(mx, sacc[i]); }
    Mh = x32_max(mx);
  }
  STAMP(10);
  BAR();
  if (wave < 4 && lane < 32) stat[wave * 32 + lane] = Mh;
  BAR();
  if (wave < 4) {
    Mh = fmaxf(fmaxf(stat[l31], stat[32 + l31]), fmaxf(stat[64 + l31], stat[96 + l31]));
    float ls = 0.f;
#pragma unroll
    for (int i = 0; i < 16; ++i) { sacc[i] = ex2(sacc[i] - Mh); ls += sacc[i]; }
    ls = x32_sum(ls);
    if (lane < 32) stat[128 + wave * 32 + lane] = ls;
#pragma unroll
    for (int g = 0; g < 4; ++g) {
      const u32x2 pv = {pk2(sacc[4 * g], sacc[4 * g + 1]), pk2(sacc[4 * g + 2], sacc[4 * g + 3])};
      *(u32x2*)(smem + QOFF + off_b(l31, 4 * wave + g) + 8 * hf) = pv;
    }
  }
  BAR();
  STAMP(11);
  const float Lh = (stat[128 + l31] + stat[160 + l31]) + (stat[192 + l31] + stat[224 + l31]);
  {
    f32x16 oacc;
#pragma unroll
    for (int i = 0; i < 16; ++i) oacc[i] = 0.f;
    const u32 cb = wave >> 2, ct = wave & 3;
    const u32 blk = (lane >> 4) & 1, q4 = (lane & 15) >> 2, p4 = lane & 3;
    u32x4 av[8], bv[8];
#pragma unroll
    for (int ks = 0; ks < 8; ++ks) {
      av[ks] = tr_pair(smem, cb * 32768 + off_b(16 * ks + 8 * hf + q4, 4 * ct + 2 * blk + (p4 >> 1)) + 8 * (p4 & 1),
                             cb * 32768 + off_b(16 * ks + 8 * hf + 4 + q4, 4 * ct + 2 * blk + (p4 >> 1)) + 8 * (p4 & 1));
      bv[ks] = *(const u32x4*)(smem + QOFF + off_b(l31, 2 * ks + hf));
    }
    SCHED();
#pragma unroll
    for (int ks = 0; ks < 8; ++ks) oacc = mfma32(av[ks], bv[ks], oacc);
    const float li = 1.f / Lh;
    STAMP(12);
    BAR();
#pragma unroll
    for (int g = 0; g < 4; ++g) {
      const u32x2 ov = {pk2(oacc[4 * g] * li, oacc[4 * g + 1] * li), pk2(oacc[4 * g + 2] * li, oacc[4 * g + 3] * li)};
      *(u32x2*)(smem + 81920 + l31 * 1040 + 64 * wave + 16 * g + 8 * hf) = ov;
    }
  }
  BAR();
  STAMP(13);
#pragma unroll
  for (int u = 0; u < 2; ++u) {
    const int h = 2 * wave + u;
    const u32x4 v = *(const u32x4*)(smem + 81920 + h * 1040 + lane * 16);
    st_sc1(rs, WS_OLAT + (u32)(((h * 2 + b) * 32 + s) * 1024 + lane * 16), v);
  }
  if (t < 32) st8_sc1(p.ws, WS_ML + (u32)(((t * 2 + b) * 32 + s) * 8), ((u64)__float_as_uint(Lh) << 32) | __float_as_uint(Mh));
  STAMP(14);
  DRAIN();
  STAMP(15);
  BAR();
  if (t < 64) st8_sc1(p.ws, WS_F2FLAG2 + t * 512 + j * 8, tag);
  STAMP(4);
  if (wave >= 8) { wo_dma(p.woA + (size_t)(64 * j) * 576, lds0, wave - 8, lane); DRAIN(); }
  float* ag = (float*)(smem + QOFF);
  constexpr u32 ZIMG = QOFF + 4096;
  if (t < 264) {
    const f32x2 v = spin_pair(rs, WS_AG + (u32)(j * 8 * 66 * 8 + t * 16), tag32);
    *(f32x2*)(ag + 2 * t) = v;
  }
  STAMP(5);
  BAR();
  if (t < 128) {
    const int bb = t >> 6, d = t & 63;
    const float* a = ag + bb * 4 * 66;
    float M = a[64];
#pragma unroll
    for (int u = 1; u < 4; ++u) M = fmaxf(M, a[u * 66 + 64]);
    float L = 0.f, z = 0.f;
#pragma unroll
    for (int u = 0; u < 4; ++u) { const float wu = ex2(a[u * 66 + 64] - M); L = fmaf(wu, a[u * 66 + 65], L); z = fmaf(wu, a[u * 66 + d], z); }
    z /= L;
    z_store(smem, ZIMG, bb, d, z);
  }
  BAR();
  oproj_mfma(smem, 0, ZIMG, rs, oslot, tag32, wave, lane);
  STAMP(6);
}

DI void role_F1(const Params& p, unsigned char* smem, __amdgpu_buffer_rsrc_t rs, u64 epoch, int w) {
  const int t = threadIdx.x, lane = t & 63, wave = __builtin_amdgcn_readfirstlane(t >> 6);
  const u64 t_start = __builtin_amdgcn_s_memrealtime();
  const int h = w & 31, dh = w >> 5;
  const int col0 = h * 128 + dh * 64;
  constexpr u32 UV_OFF = 0, WO_OFF = 65536, AI_OFF = 139264, SM_OFF = 155648;
  constexpr u32 ZIMG = SM_OFF + 2048;
  float* red = (float*)(smem + AI_OFF);
  unsigned char* qsh = smem + AI_OFF + 5120;
  float* qas = (float*)(smem + AI_OFF + 8192);
  float* part = (float*)(smem + SM_OFF);
  float* gsh = (float*)(smem + SM_OFF + 2560);
  float* fin = (float*)(smem + SM_OFF + 3072);
  const u32 lds0 = (u32)(size_t)smem;
  const u32 oslot = WS_OG + (u32)((64 + w) * 9216);
  u32x4 uk[4];
  const int ch = t & 7;
  if (wave < 9) {
    Gemv1 g;
    gemv1_issue(g, p.wqB + col0, p.x, t);
    WAITV(4);
#pragma unroll
    for (int k = 0; k < 4; ++k) uk[k] = ldg16nt(p.wuk + (size_t)((t >> 3) + 128 * k) * 4096 + col0 + 8 * ch);
    STAMP(8);
    gemv1_finish(g, red, wave, lane);
    STAMP(9);
  } else {
    __builtin_amdgcn_s_sleep(UK_DELAY);
#pragma unroll
    for (int k = 0; k < 4; ++k) uk[k] = ldg16nt(p.wuk + (size_t)((t >> 3) + 128 * k) * 4096 + col0 + 8 * ch);
    if (wave >= 14) {
      const u16* xr = p.x + (wave - 14) * 576;
      float sg = 0.f;
#pragma unroll
      for (int k = 0; k < 9; ++k) {
        const int mm = lane + 64 * k;
        sg = fmaf(__uint_as_float((u32)xr[mm] << 16), __uint_as_float((u32)p.gw[mm] << 16), sg);
      }
      sg += dpp<XOR1>(sg); sg += dpp<XOR2>(sg); sg += dpp<ROR4>(sg); sg += dpp<ROR8>(sg);
      sg = x32_sum(x16_sum(sg));
      if (lane == 0) gsh[wave - 14] = 1.f / (1.f + __expf(-sg));
    }
  }
  const u64 tag = tag_of(epoch);
  const u32 tag32 = (u32)tag;
  BAR();
  if (t < 16) *(u32x4*)(qsh + t * 16) = gemv1_sum(red, t);
  BAR();
  {
    const u32x4 q0 = *(const u32x4*)(qsh + ch * 16), q1 = *(const u32x4*)(qsh + 128 + ch * 16);
    float x[8];
#pragma unroll
    for (int k = 0; k < 4; ++k) { x[2 * k] = dot8(uk[k], q0); x[2 * k + 1] = dot8(uk[k], q1); }
    const int c = ch;
    TR_STEP(4, XOR7, 4) TR_STEP(2, XOR2, 2) TR_STEP(1, XOR1, 1)
    qas[(ch & 1) * 512 + (t >> 3) + 128 * (ch >> 1)] = x[0];
  }
  STAMP(10);
  BAR();
  if (wave < 2) {
    const f32x4 v0 = *(const f32x4*)(qas + t * 8), v1 = *(const f32x4*)(qas + t * 8 + 4);
    const u32x4 o = {pk2(v0[0], v0[1]), pk2(v0[2], v0[3]), pk2(v1[0], v1[1]), pk2(v1[2], v1[3])};
    st_sc1(rs, WS_QABS + (u32)((((t >> 6) * 2 + dh) * 32 + h) * 1024 + (t & 63) * 16), o);
    DRAIN();
  }
  BAR();
  if (t < 64) st8_sc1(p.ws, WS_F1FLAG + t * 512 + w * 8, tag);
  STAMP(1);
  float* rst = (float*)(smem + SM_OFF + 2624);
  if (t < 576) st_sc1(rs, oslot + t * 16, u32x4{0u, ~tag32, 0u, ~tag32});
  DRAIN();
  BAR();
  if (t == 0) st8_sc1(p.ws, WS_F1FLAGC + w * 8, tag);
  if (wave < 8) {
    while (__builtin_amdgcn_s_memrealtime() - t_start < (u64)LATE_DMA) __builtin_amdgcn_s_sleep(4);
    wo_dma(p.woB + (size_t)col0 * 576, lds0 + WO_OFF, wave, lane);
  } else {
    while (__builtin_amdgcn_s_memrealtime() - t_start < (u64)LATE_UV) __builtin_amdgcn_s_sleep(4);
#pragma unroll
    for (int it = 0; it < 8; ++it) {
      const int n = (wave - 8) * 8 + it;
      const u32 row = n * 8 + (lane >> 3);
      glds16(p.wuv + (size_t)row * 4096 + col0 + (((lane & 7) ^ wsw(row)) << 3), lds0 + UV_OFF + n * 1024);
    }
  }
  STAMP(2);
  {
    const int bb = wave >> 3, sg = wave & 7;
    if (wave == 0) poll_flags(p.ws, WS_F1FLAGC + lane * 8, true, tag);
    poll_flags(p.ws, WS_F2FLAG2 + w * 512 + (2 * (4 * sg + (lane & 3)) + bb) * 8, lane < 4, tag);
    u32x4 pv[4];
#pragma unroll
    for (int u = 0; u < 4; ++u) pv[u] = ld_sc1(rs, WS_OLAT + (u32)(((h * 2 + bb) * 32 + 4 * sg + u) * 1024 + lane * 16));
    const u64 ml = ld8_sc1(p.ws, WS_ML + (u32)(((h * 2 + bb) * 32 + 4 * sg + (lane & 3)) * 8));
    const float ms = __uint_as_float((u32)ml), ls = __uint_as_float((u32)(ml >> 32));
    float M = ms;
    M = fmaxf(M, dpp<XOR1>(M)); M = fmaxf(M, dpp<XOR2>(M));
    const float wl = ls * ex2(ms - M);
    float L = wl;
    L += dpp<XOR1>(L); L += dpp<XOR2>(L);
    if (lane == 0) *(f32x2*)(rst + wave * 2) = f32x2{M, L};
    float a[8];
#pragma unroll
    for (int e = 0; e < 8; ++e) a[e] = 0.f;
#pragma unroll
    for (int u = 0; u < 4; ++u) {
      const float wu = u == 0 ? dpp<0x00>(wl) : u == 1 ? dpp<0x55>(wl) : u == 2 ? dpp<0xAA>(wl) : dpp<0xFF>(wl);
#pragma unroll
      for (int e = 0; e < 8; ++e) a[e] = fmaf(wu, (e & 1) ? bfhi(pv[u][e >> 1]) : bflo(pv[u][e >> 1]), a[e]);
    }
    *(u32x4*)(smem + AI_OFF + (lane >> 4) * 4096 + off_b(wave, lane & 15)) =
        u32x4{pk2(a[0], a[1]), pk2(a[2], a[3]), pk2(a[4], a[5]), pk2(a[6], a[7])};
    DRAIN();
  }
  STAMP(3);
  BAR();
  {
    const int nt = wave & 3, kq = wave >> 2;
    u32x4 af[4], bf[4];
#pragma unroll
    for (int u = 0; u < 4; ++u) {
      af[u] = *(const u32x4*)(smem + AI_OFF + kq * 4096 + off_b(lane & 15, 4 * u + (lane >> 4)));
      bf[u] = wfrag(smem, UV_OFF, 128, 4 * kq + u, nt, lane);
    }
    const float* rb = rst + (lane >> 5) * 16;
    float M = rb[0];
#pragma unroll
    for (int u = 1; u < 8; ++u) M = fmaxf(M, rb[2 * u]);
    float L = 0.f;
#pragma unroll
    for (int u = 0; u < 8; ++u) L = fmaf(ex2(rb[2 * u] - M), rb[2 * u + 1], L);
    const float* rr = rst + (lane >> 4) * 8;
    const float li = 1.f / L;
    f32x4 acc = {0.f, 0.f, 0.f, 0.f};
#pragma unroll
    for (int u = 0; u < 4; ++u) acc = mfma16(af[u], bf[u], acc);
    float sv = 0.f;
#pragma unroll
    for (int u = 0; u < 4; ++u) sv = fmaf(acc[u], ex2(rr[2 * u] - M), sv);
    sv = x16_sum(sv) * li;
    if ((lane & 16) == 0) part[(kq * 2 + (lane >> 5)) * 64 + nt * 16 + (lane & 15)] = sv;
  }
  STAMP(12);
  BAR();
  if (t < 128) z_store(smem, ZIMG, t >> 6, t & 63, (part[t] + part[128 + t]) + (part[256 + t] + part[384 + t]));
  BAR();
  STAMP(13);
  oproj_mfma(smem, WO_OFF, ZIMG, rs, oslot, tag32, wave, lane);
  STAMP(4);
  if (t < 576) {
    const int pp = t / 9, i = t - pp * 9;
    const u32 off = WS_OG + (u32)(pp * 9216 + (9 * w + i) * 16);
    const f32x2 vb = spin_pair(rs, off + 64 * 9216, tag32);
    STAMP(5);
    const f32x2 va = spin_pair(rs, off, tag32);
    const float g0 = gsh[0], g1 = gsh[1];
    *(f32x2*)(fin + 2 * t) = f32x2{g0 * va[0] + (1.f - g0) * vb[0], g1 * va[1] + (1.f - g1) * vb[1]};
  }
  STAMP(6);
  BAR();
  if (t < 192) {
    const int o = t >> 3, k = t & 7;
    float sum = 0.f;
    if (o < 18) {
#pragma unroll
      for (int u = 0; u < 8; ++u) sum += fin[(k * 8 + u) * 18 + o];
    }
    sum += dpp<XOR1>(sum); sum += dpp<XOR2>(sum); sum += dpp<XOR7>(sum);
    if (k == 0 && o < 18) p.out[(o & 1) * 576 + 9 * w + (o >> 1)] = bf1(sum);
    if (w == 0 && t == 0) st8_sc1(p.ws, WS_EPOCH, tag);
  }
  STAMP(7);
}

__global__ void __launch_bounds__(1024, 4) fused_kernel(Params p) {
  __shared__ __attribute__((aligned(16))) unsigned char smem[LDS_BYTES];
  const u64 tag = __hip_atomic_load((GAS u64*)(p.ws + WS_EPOCH), __ATOMIC_RELAXED, __HIP_MEMORY_SCOPE_WORKGROUP);
  STAMP(0);
  const __amdgpu_buffer_rsrc_t rs = __builtin_amdgcn_make_buffer_rsrc(p.ws, 0, WS_END, 0x00020000);
  const int w = blockIdx.x;
  if (w < 64) role_F1(p, smem, rs, tag, w);
  else if (w < 128) role_F2(p, smem, rs, tag, w - 64);
  else {
    role_A(p, smem, rs, tag, w - 128);
  }
}

}

extern "C" void kernel_launch(void* const* d_in, const int* in_sizes, int n_in,
                              void* d_out, int out_size, void* d_ws, size_t ws_size,
                              hipStream_t stream) {
  Params p;
  p.x = (const u16*)d_in[0];
  p.K = (const u16*)d_in[1];
  p.V = (const u16*)d_in[2];
  p.wqA = (const u16*)d_in[3];
  p.woA = (const u16*)d_in[4];
  p.ckv = (const u16*)d_in[6];
  p.wqB = (const u16*)d_in[7];
  p.wuk = (const u16*)d_in[8];
  p.wuv = (const u16*)d_in[9];
  p.woB = (const u16*)d_in[10];
  p.gw = (const u16*)d_in[11];
  p.out = (u16*)d_out;
  p.ws = (unsigned char*)d_ws;
  hipLaunchKernelGGL(fused_kernel, dim3(256), dim3(1024), 0, stream, p);
}
```

```cpp
#include <hip/hip_runtime.h>
#include <hip/hip_bf16.h>
#include <stdint.h>

namespace {

typedef unsigned short u16;
typedef unsigned int u32;
typedef unsigned long long u64;
typedef u32 u32x4 __attribute__((ext_vector_type(4)));
typedef u32 u32x2 __attribute__((ext_vector_type(2)));
typedef float f32x4 __attribute__((ext_vector_type(4)));
typedef float f32x2 __attribute__((ext_vector_type(2)));
typedef float f32x16 __attribute__((ext_vector_type(16)));
typedef short s16x4 __attribute__((ext_vector_type(4)));
typedef __bf16 bf16x2 __attribute__((ext_vector_type(2)));
typedef __bf16 bf16x8 __attribute__((ext_vector_type(8)));
#define GAS __attribute__((address_space(1)))
#define LAS __attribute__((address_space(3)))
#define RLX_AGENT __ATOMIC_RELAXED, __HIP_MEMORY_SCOPE_AGENT
#define DI __device__ __forceinline__

constexpr u32 WS_EPOCH = 0;
constexpr u32 WS_AFLAG = 3072;
constexpr u32 WS_F1FLAGC = 4096;
constexpr u32 WS_QA = 8192;
constexpr u32 WS_QABS = 8192 + 65536;
constexpr u32 WS_ML = WS_QABS + 131072;
constexpr u32 WS_OLAT = WS_ML + 16384;
constexpr u32 WS_AG = WS_OLAT + 2097152;
constexpr u32 WS_OG = WS_AG + 64 * 8 * 66 * 8;
constexpr u32 WS_F1FLAG = WS_OG + 2 * 64 * 576 * 16;
constexpr u32 WS_F2FLAG2 = WS_F1FLAG + 32768;
constexpr u32 WS_END = WS_F2FLAG2 + 32768;

constexpr int LDS_BYTES = 163840;
constexpr u32 QOFF = 131072;
constexpr float C2 = 0.08838834764831845f * 1.4426950408889634f;
constexpr u32 SPIN_CAP = 1u << 18;
constexpr int A_SLOW = 3;
constexpr int DMA_EARLY = 4;
constexpr u64 LATE_DMA = 1200;
constexpr int POLL_SLEEP = 4;
#define WAITV(n) asm volatile("s_waitcnt vmcnt(" #n ")" ::: "memory")

#define BAR() asm volatile("s_waitcnt lgkmcnt(0)\n\ts_barrier" ::: "memory")
#define DRAIN() asm volatile("s_waitcnt vmcnt(0)" ::: "memory")
#define SCHED() __builtin_amdgcn_sched_barrier(0)

struct Params {
  const u16 *x, *K, *V, *wqA, *woA, *ckv, *wqB, *wuk, *wuv, *woB, *gw;
  u16* out;
  unsigned char* ws;
};

DI float bflo(u32 v) { return __uint_as_float(v << 16); }
DI float bfhi(u32 v) { return __uint_as_float(v & 0xffff0000u); }
DI u32 pk2(float lo, float hi) {
  bf16x2 r; r[0] = (__bf16)lo; r[1] = (__bf16)hi; return __builtin_bit_cast(u32, r);
}
DI u16 bf1(float v) { const __bf16 r = (__bf16)v; return __builtin_bit_cast(u16, r); }
DI float dot2(u32 a, u32 b, float acc) {
  return __builtin_amdgcn_fdot2_f32_bf16(__builtin_bit_cast(bf16x2, a), __builtin_bit_cast(bf16x2, b), acc, false);
}
DI float dot8(u32x4 a, u32x4 b) {
  float d = dot2(a[0], b[0], 0.f); d = dot2(a[1], b[1], d); d = dot2(a[2], b[2], d); return dot2(a[3], b[3], d);
}
template <int CTRL> DI float dpp(float x) {
  return __builtin_bit_cast(float, __builtin_amdgcn_mov_dpp(__builtin_bit_cast(int, x), CTRL, 0xf, 0xf, true));
}
constexpr int XOR1 = 0xB1, XOR2 = 0x4E, XOR7 = 0x141, ROR8 = 0x128, ROR4 = 0x124;
DI float tr32(float a, float b) {
  auto s = __builtin_amdgcn_permlane32_swap(__float_as_uint(a), __float_as_uint(b), false, false);
  return __uint_as_float(s[0]) + __uint_as_float(s[1]);
}
DI float tr16(float a, float b) {
  auto s = __builtin_amdgcn_permlane16_swap(__float_as_uint(a), __float_as_uint(b), false, false);
  return __uint_as_float(s[0]) + __uint_as_float(s[1]);
}
DI float x16_max(float x) {
  auto s = __builtin_amdgcn_permlane16_swap(__float_as_uint(x), __float_as_uint(x), false, false);
  return fmaxf(__uint_as_float(s[0]), __uint_as_float(s[1]));
}
DI float x32_max(float x) {
  auto s = __builtin_amdgcn_permlane32_swap(__float_as_uint(x), __float_as_uint(x), false, false);
  return fmaxf(__uint_as_float(s[0]), __uint_as_float(s[1]));
}
DI float x16_sum(float x) { return tr16(x, x); }
DI float x32_sum(float x) { return tr32(x, x); }
DI float ex2(float x) { return __builtin_amdgcn_exp2f(x); }

DI u32x4 ldg16(const u16* p) { return *(const GAS u32x4*)p; }
DI u32x4 ldg16nt(const u16* p) { return __builtin_nontemporal_load((const GAS u32x4*)p); }
DI u32x4 ld_sc1(__amdgpu_buffer_rsrc_t rs, u32 off) { return __builtin_amdgcn_raw_buffer_load_b128(rs, off, 0, 16); }
DI void st_sc1(__amdgpu_buffer_rsrc_t rs, u32 off, u32x4 v) { __builtin_amdgcn_raw_buffer_store_b128(v, rs, off, 0, 16); }
DI u64 ld8_sc1(unsigned char* ws, u32 off) { return __hip_atomic_load((GAS u64*)(ws + off), RLX_AGENT); }
DI void st8_sc1(unsigned char* ws, u32 off, u64 v) { __hip_atomic_store((GAS u64*)(ws + off), v, RLX_AGENT); }

DI u64 mix64(u64 e) {
  u64 z = e + 0x9E3779B97F4A7C15ull;
  z = (z ^ (z >> 30)) * 0xBF58476D1CE4E5B9ull;
  z = (z ^ (z >> 27)) * 0x94D049BB133111EBull;
  return z ^ (z >> 31);
}
DI u64 tag_of(u64 e) {
  u32 lo = (u32)e, hi = (u32)(e >> 32);
  asm volatile("" : "+v"(lo), "+v"(hi) :: "memory");
  lo = __builtin_amdgcn_readfirstlane(lo); hi = __builtin_amdgcn_readfirstlane(hi);
  return mix64(((u64)hi << 32) | lo);
}
DI void poll_flags(unsigned char* ws, u32 off, bool active, u64 tag) {
  for (u32 spins = 0; spins < SPIN_CAP; ++spins) {
    bool ok = true;
    if (active) ok = (ld8_sc1(ws, off) == tag);
    if (__all(ok)) break;
    __builtin_amdgcn_s_sleep(POLL_SLEEP);
  }
  __builtin_amdgcn_fence(__ATOMIC_ACQUIRE, "wavefront");
}
DI f32x2 spin_pair(__amdgpu_buffer_rsrc_t rs, u32 off, u32 tag32) {
  u32x4 v;
  for (u32 spins = 0; spins < SPIN_CAP; ++spins) {
    asm volatile("" ::: "memory");
    v = ld_sc1(rs, off);
    bool ok = (v[1] == tag32) & (v[3] == tag32);
    if (__all(ok)) break;
  }
  f32x2 r; r[0] = __uint_as_float(v[0]); r[1] = __uint_as_float(v[2]); return r;
}
DI void glds16(const void* gsrc, u32 lds_dst) {
  u32 keep;
  asm volatile("s_mov_b32 %0, m0\n\ts_mov_b32 m0, %2\n\ts_nop 0\n\tglobal_load_lds_dwordx4 %1, off nt\n\ts_mov_b32 m0, %0"
               : "=&s"(keep) : "v"(gsrc), "s"(lds_dst) : "memory");
}
DI u32 off_b(u32 row, u32 ch) { return 256u * row + 16u * (ch ^ (((row & 3) << 2) | ((row >> 2) & 3))); }
DI u32 wsw(u32 row) { return (((row >> 1) & 1) | (((row >> 3) & 1) << 1)) << 1; }
DI f32x4 mfma16(u32x4 a, u32x4 b, f32x4 c) {
  return __builtin_amdgcn_mfma_f32_16x16x32_bf16(__builtin_bit_cast(bf16x8, a), __builtin_bit_cast(bf16x8, b), c, 0, 0, 0);
}
DI f32x16 mfma32(u32x4 a, u32x4 b, f32x16 c) {
  return __builtin_amdgcn_mfma_f32_32x32x16_bf16(__builtin_bit_cast(bf16x8, a), __builtin_bit_cast(bf16x8, b), c, 0, 0, 0);
}
DI u32x4 tr_pair(const unsigned char* smem, u32 a0, u32 a1) {
  const s16x4 lo = __builtin_amdgcn_ds_read_tr16_b64_v4i16((LAS s16x4*)(smem + a0));
  const s16x4 hi = __builtin_amdgcn_ds_read_tr16_b64_v4i16((LAS s16x4*)(smem + a1));
  return __builtin_bit_cast(u32x4, __builtin_shufflevector(lo, hi, 0, 1, 2, 3, 4, 5, 6, 7));
}
DI u32x4 wfrag(const unsigned char* smem, u32 img, u32 stride, u32 ks, u32 ct, int lane) {
  const u32 g = lane >> 4, q = (lane & 15) >> 2, pp = lane & 3;
  const u32 r0 = 32 * ks + 8 * g + q, r1 = r0 + 4;
  const u32 ch = 2 * ct + (pp >> 1);
  return tr_pair(smem, img + r0 * stride + ((ch ^ wsw(r0)) << 4) + 8 * (pp & 1),
                       img + r1 * stride + ((ch ^ wsw(r1)) << 4) + 8 * (pp & 1));
}


struct Gemv1 { u32x4 w[8]; u32x4 x0, x1; };
DI void gemv1_issue(Gemv1& g, const u16* W, const u16* x, int t) {
  const int rg = t >> 3, ch = t & 7;
  const u16* wp = W + (size_t)(8 * rg) * 4096 + 8 * ch;
  g.x0 = ldg16(x + 8 * rg);
  g.x1 = ldg16(x + 576 + 8 * rg);
#pragma unroll
  for (int k = 0; k < 8; ++k) g.w[k] = ldg16nt(wp + (size_t)k * 4096);
}
DI void gemv1_finish(Gemv1& g, float* red  , int wave, int lane) {
  float a0[8], a1[8];
#pragma unroll
  for (int e = 0; e < 8; ++e) { a0[e] = 0.f; a1[e] = 0.f; }
#pragma unroll
  for (int k = 0; k < 8; ++k) {
    const float xa = (k & 1) ? bfhi(g.x0[k >> 1]) : bflo(g.x0[k >> 1]);
    const float xb = (k & 1) ? bfhi(g.x1[k >> 1]) : bflo(g.x1[k >> 1]);
#pragma unroll
    for (int e = 0; e < 8; ++e) {
      const float wf = (e & 1) ? bfhi(g.w[k][e >> 1]) : bflo(g.w[k][e >> 1]);
      a0[e] = fmaf(xa, wf, a0[e]); a1[e] = fmaf(xb, wf, a1[e]);
    }
  }
  float v[8], u[4], wv[2];
#pragma unroll
  for (int e = 0; e < 8; ++e) v[e] = tr32(a0[e], a1[e]);
#pragma unroll
  for (int e = 0; e < 4; ++e) u[e] = tr16(v[e], v[e + 4]);
#pragma unroll
  for (int e = 0; e < 2; ++e) {
    const float t1 = u[e] + dpp<ROR8>(u[e]), t2 = u[e + 2] + dpp<ROR8>(u[e + 2]);
    wv[e] = (lane & 8) ? t2 : t1;
  }
  *(f32x2*)(red + wave * 128 + (lane >> 5) * 64 + (lane & 7) * 8 + ((lane >> 3) & 1) * 2 + ((lane >> 4) & 1) * 4) = f32x2{wv[0], wv[1]};
}
DI u32x4 gemv1_sum(const float* red, int t) {
  f32x4 s0 = {0.f, 0.f, 0.f, 0.f}, s1 = {0.f, 0.f, 0.f, 0.f};
  const float* r = red + (t >> 3) * 64 + (t & 7) * 8;
#pragma unroll
  for (int v = 0; v < 9; ++v) { s0 += *(const f32x4*)(r + v * 128); s1 += *(const f32x4*)(r + v * 128 + 4); }
  return u32x4{pk2(s0[0], s0[1]), pk2(s0[2], s0[3]), pk2(s1[0], s1[1]), pk2(s1[2], s1[3])};
}
DI void wo_dma(const u16* W, u32 lds_img, int wv, int lane) {
#pragma unroll 3
  for (int it = 0; it < 9; ++it) {
    const int n = wv * 9 + it;
    const u32 P = n * 1024 + lane * 16, row = P / 1152, cpos = (P - row * 1152) >> 4;
    glds16(W + (size_t)row * 576 + ((cpos ^ wsw(row)) << 3), lds_img + n * 1024);
  }
}
DI void oproj_mfma(const unsigned char* smem, u32 img, u32 zimg, __amdgpu_buffer_rsrc_t rs, u32 slot, u32 tag32, int wave, int lane) {
  u32x4 a[2];
#pragma unroll
  for (int ks = 0; ks < 2; ++ks) {
    a[ks] = u32x4{0u, 0u, 0u, 0u};
    if ((lane & 15) < 4) a[ks] = *(const u32x4*)(smem + zimg + (lane & 15) * 128 + (32 * ks + 8 * (lane >> 4)) * 2);
  }
  const int nt = wave < 4 ? 3 : 2;
  u32x4 bfr[3][2];
#pragma unroll
  for (int i = 0; i < 3; ++i)
    if (i < nt) {
#pragma unroll
      for (int ks = 0; ks < 2; ++ks) bfr[i][ks] = wfrag(smem, img, 1152, ks, wave + 16 * i, lane);
    }
#pragma unroll
  for (int i = 0; i < 3; ++i)
    if (i < nt) {
      f32x4 acc = {0.f, 0.f, 0.f, 0.f};
      acc = mfma16(a[0], bfr[i][0], acc);
      acc = mfma16(a[1], bfr[i][1], acc);
      if (lane < 16)
        st_sc1(rs, slot + (u32)(((wave + 16 * i) * 16 + lane) * 16),
               u32x4{__float_as_uint(acc[0] + acc[2]), tag32, __float_as_uint(acc[1] + acc[3]), tag32});
    }
}
DI void z_store(unsigned char* smem, u32 zimg, int b, int d, float z) {
  const u16 hi = bf1(z);
  const u16 lo = bf1(z - __uint_as_float((u32)hi << 16));
  *(u16*)(smem + zimg + b * 128 + d * 2) = hi;
  *(u16*)(smem + zimg + (2 + b) * 128 + d * 2) = lo;
}

#define TR_STEP(HALF, CTRL, BIT)                                                          \
  _Pragma("unroll") for (int h_ = 0; h_ < (HALF); ++h_) {                                 \
    const float lo_ = x[h_], hi_ = x[h_ + (HALF)];                                        \
    const float t1_ = lo_ + dpp<CTRL>(lo_), t2_ = hi_ + dpp<CTRL>(hi_);                   \
    x[h_] = (c & (BIT)) ? t2_ : t1_;                                                      \
  }

constexpr int NLA = 16;
DI void ag_clear(__amdgpu_buffer_rsrc_t rs, int hkv, int bs, u32 ntag, int lane) {
#pragma unroll
  for (int k = 0; k < 3; ++k) {
    const int n = lane + 64 * k;
    if (n < 132) {
      const int seg = n / 33, pos = n - seg * 33;
      st_sc1(rs, WS_AG + (u32)((((hkv * 4 + seg) * 8 + bs) * 66 + 2 * pos) * 8), u32x4{0u, ntag, 0u, ntag});
    }
  }
}
DI void q_fetch(__amdgpu_buffer_rsrc_t rs, unsigned char* qs, int b, int hkv, u64 tag, int lane) {
  const u32 thi = (u32)(tag >> 32), tlo = (u32)tag & 0xffff0000u;
  const u32 off = WS_QA + (u32)((b * 4096 + hkv * 256 + lane * 4) * 8);
  u32x4 g0, g1;
  for (u32 spins = 0; spins < SPIN_CAP; ++spins) {
    asm volatile("" ::: "memory");
    g0 = ld_sc1(rs, off); g1 = ld_sc1(rs, off + 16);
    const bool ok = (g0[1] == thi) & (g0[3] == thi) & (g1[1] == thi) & (g1[3] == thi) &
                    ((g0[0] & 0xffff0000u) == tlo) & ((g0[2] & 0xffff0000u) == tlo) &
                    ((g1[0] & 0xffff0000u) == tlo) & ((g1[2] & 0xffff0000u) == tlo);
    if (__all(ok)) break;
    __builtin_amdgcn_s_sleep(1);
  }
  *(u32x2*)(qs + lane * 8) = u32x2{(g0[0] & 0xffffu) | (g0[2] << 16), (g1[0] & 0xffffu) | (g1[2] << 16)};
}
template <int NL>
DI void gqa_wave(u32x4 (&kv)[NL], const u16* Vp, const unsigned char* qs, float* pw, float* mrg, float* mls, int wave, int lane) {
  constexpr int NG = (NL + 7) / 8, NLP = NL <= 4 ? 4 : 8 * NG;
  const int r = lane >> 4, c = lane & 15;
  const u32x4 q0 = *(const u32x4*)(qs + c * 16), q1 = *(const u32x4*)(qs + 256 + c * 16);
  float sc[NG];
#pragma unroll
  for (int G = 0; G < NG; ++G) {
    float x[16];
#pragma unroll
    for (int ii = 0; ii < 8; ++ii) {
      const int i = 8 * G + ii;
      if (i < NL) {
        x[2 * ii] = dot8(kv[i], q0);
        x[2 * ii + 1] = dot8(kv[i], q1);
        SCHED();
        kv[i] = ldg16nt(Vp + i * 512);
        SCHED();
      } else { x[2 * ii] = 0.f; x[2 * ii + 1] = 0.f; }
    }
    TR_STEP(8, ROR8, 8) TR_STEP(4, XOR7, 4) TR_STEP(2, XOR2, 2) TR_STEP(1, XOR1, 1)
    sc[G] = (8 * G + (c >> 1) < NL) ? x[0] * C2 : -INFINITY;
  }
  float m = sc[0];
#pragma unroll
  for (int G = 1; G < NG; ++G) m = fmaxf(m, sc[G]);
  m = fmaxf(m, dpp<XOR2>(m)); m = fmaxf(m, dpp<ROR4>(m)); m = fmaxf(m, dpp<ROR8>(m));
  m = x32_max(x16_max(m));
  float l = 0.f;
#pragma unroll
  for (int G = 0; G < NG; ++G) {
    const float pe = ex2(sc[G] - m);
    l += pe;
    if (16 * G + c < 2 * NLP) pw[r * 2 * NLP + 16 * G + c] = pe;
  }
  l += dpp<XOR2>(l); l += dpp<ROR4>(l); l += dpp<ROR8>(l);
  l = x32_sum(x16_sum(l));
  float a0[8], a1[8];
#pragma unroll
  for (int e = 0; e < 8; ++e) { a0[e] = 0.f; a1[e] = 0.f; }
#pragma unroll
  for (int i2 = 0; i2 < (NL + 1) / 2; ++i2) {
    const f32x4 pp = *(const f32x4*)(pw + r * 2 * NLP + i2 * 4);
#pragma unroll
    for (int ii = 0; ii < 2; ++ii) {
      const int i = 2 * i2 + ii;
      if (i < NL) {
        const float pa = pp[2 * ii], pb = pp[2 * ii + 1];
#pragma unroll
        for (int e = 0; e < 8; ++e) {
          const float vf = (e & 1) ? bfhi(kv[i][e >> 1]) : bflo(kv[i][e >> 1]);
          a0[e] = fmaf(pa, vf, a0[e]); a1[e] = fmaf(pb, vf, a1[e]);
        }
      }
    }
  }
  float v[8];
#pragma unroll
  for (int e = 0; e < 8; ++e) v[e] = tr32(a0[e], a1[e]);
  f32x4 u;
#pragma unroll
  for (int e = 0; e < 4; ++e) u[e] = tr16(v[e], v[e + 4]);
  *(f32x4*)(mrg + wave * 256 + (lane >> 5) * 128 + c * 8 + ((lane >> 4) & 1) * 4) = u;
  if (lane < 2) *(f32x2*)(mls + wave * 4 + lane * 2) = f32x2{m, l};
}
DI void gqa_merge(unsigned char* ws, const float* mrg, const float* mls, int hkv, int bs, u32 tag32, int t) {
  const int g = t >> 7, d = t & 127;
  float mv[16], M = -INFINITY;
#pragma unroll
  for (int v = 0; v < 16; ++v) { mv[v] = mls[v * 4 + g * 2]; M = fmaxf(M, mv[v]); }
  float L = 0.f, A = 0.f;
#pragma unroll
  for (int v = 0; v < 16; ++v) {
    const float w = ex2(mv[v] - M);
    L = fmaf(w, mls[v * 4 + g * 2 + 1], L);
    A = fmaf(w, mrg[v * 256 + t], A);
  }
  const u32 slot = WS_AG + (u32)((((hkv * 2 + g) * 2 + (d >> 6)) * 8 + bs) * 66 * 8);
  st8_sc1(ws, slot + (d & 63) * 8, ((u64)tag32 << 32) | __float_as_uint(A));
  if ((d & 63) < 2) st8_sc1(ws, slot + (64 + (d & 1)) * 8, ((u64)tag32 << 32) | __float_as_uint((d & 1) ? L : M));
}

DI void role_A(const Params& p, unsigned char* smem, __amdgpu_buffer_rsrc_t rs, u64 epoch, int a) {
  const int t = threadIdx.x, lane = t & 63, wave = __builtin_amdgcn_readfirstlane(t >> 6);
  const int hkv = a & 15, b = (a >> 4) & 1, s = a >> 5;
  const size_t base = ((size_t)(b * 16 + hkv) * 4096 + s * (64 * NLA) + wave * (4 * NLA) + (lane >> 4)) * 128 + (lane & 15) * 8;
  const u16* Kp = p.K + base;
  unsigned char* qs = smem;
  float* pw = (float*)(smem + 1024) + wave * 128;
  float* mrg = (float*)(smem + 1024 + 8192);
  float* mls = mrg + 16 * 256;
  u32x4 kv[NLA];
  u64 tag;
  if (wave == 0) {
    tag = tag_of(epoch);
    ag_clear(rs, hkv, b * 4 + s, ~(u32)tag, lane);
    DRAIN();
    if (lane == 0) st8_sc1(p.ws, WS_AFLAG + a * 8, tag);
    q_fetch(rs, qs, b, hkv, tag, lane);
    BAR();
#pragma unroll
    for (int i = 0; i < NLA; ++i) kv[i] = ldg16nt(Kp + i * 512);
  } else {
#pragma unroll
    for (int i = 0; i < NLA; ++i) {
      kv[i] = ldg16nt(Kp + i * 512);
      if (i < A_SLOW) WAITV(0);
      else if (i < NLA - 1) WAITV(3);
    }
    tag = tag_of(epoch);
    BAR();
  }
  gqa_wave<NLA>(kv, p.V + base, qs, pw, mrg, mls, wave, lane);
  BAR();
  if (t < 256) gqa_merge(p.ws, mrg, mls, hkv, b * 4 + s, (u32)tag, t);
}

DI void ckv_dma(const Params& p, u32 lds0, int b, int s, int w4, int lane, int it) {
  const int n = it * 4 + w4;
  const u32 row = 4 * (n >> 2) + (lane >> 4), cb = n & 3;
  const u32 ch = (lane & 15) ^ (((row & 3) << 2) | ((row >> 2) & 3));
  glds16(p.ckv + ((size_t)(b * 4096 + s * 128 + row) * 512 + cb * 128 + ch * 8), lds0 + cb * 32768 + (n >> 2) * 1024);
}
DI void role_F2(const Params& p, unsigned char* smem, __amdgpu_buffer_rsrc_t rs, u64 epoch, int j) {
  const int t = threadIdx.x, lane = t & 63, wave = __builtin_amdgcn_readfirstlane(t >> 6);
  const int b = j & 1, s = j >> 1;
  float* red = (float*)(smem + QOFF);
  const u32 lds0 = (u32)(size_t)smem;
  const u32 oslot = WS_OG + (u32)(j * 9216);
  if (wave < 9) {
    Gemv1 g;
    gemv1_issue(g, p.wqA + 64 * j, p.x, t);
    gemv1_finish(g, red, wave, lane);
  } else if (wave >= 12) {
    for (int it = 0; it < DMA_EARLY; ++it) {
      ckv_dma(p, lds0, b, s, wave - 12, lane, it);
      WAITV(1);
    }
  }
  const u64 tag = tag_of(epoch);
  const u32 tag32 = (u32)tag;
  BAR();
  if (wave == 0) {
    f32x2 sq = {0.f, 0.f};
#pragma unroll
    for (int v = 0; v < 9; ++v) sq += *(const f32x2*)(red + v * 128 + 2 * lane);
    const u32 thi = (u32)(tag >> 32), tlo = (u32)tag & 0xffff0000u;
    st_sc1(rs, WS_QA + (u32)(((lane >> 5) * 4096 + 64 * j + (lane & 31) * 2) * 8),
           u32x4{tlo | bf1(sq[0]), thi, tlo | bf1(sq[1]), thi});
  }
  BAR();
  if (wave < 9) {
    st_sc1(rs, oslot + t * 16, u32x4{0u, ~tag32, 0u, ~tag32});
  } else if (wave == 9) {
    poll_flags(p.ws, WS_AFLAG + ((j >> 2) + 16 * (lane & 7)) * 8, lane < 8, tag);
  } else if (wave >= 12) {
#pragma unroll 4
    for (int it = DMA_EARLY; it < 32; ++it) { ckv_dma(p, lds0, b, s, wave - 12, lane, it); WAITV(7); }
  }
  {
    poll_flags(p.ws, WS_F1FLAG + j * 512 + (wave + 16 * (lane & 3)) * 8, lane < 4, tag);
    u32x4 v0[2], v1[2];
#pragma unroll
    for (int u = 0; u < 2; ++u) {
      const int n = t + 1024 * u;
      v0[u] = ld_sc1(rs, WS_QABS + (u32)((b * 2 + 0) * 32768 + n * 16));
      v1[u] = ld_sc1(rs, WS_QABS + (u32)((b * 2 + 1) * 32768 + n * 16));
    }
#pragma unroll
    for (int u = 0; u < 2; ++u) {
      const int n = t + 1024 * u;
      u32x4 o;
#pragma unroll
      for (int e = 0; e < 4; ++e) o[e] = pk2(bflo(v0[u][e]) + bflo(v1[u][e]), bfhi(v0[u][e]) + bfhi(v1[u][e]));
      const u32 h = n >> 6, c8 = n & 63;
      *(u32x4*)(smem + QOFF + (c8 >> 4) * 8192 + off_b(h, c8 & 15)) = o;
    }
    DRAIN();
  }
  BAR();
  const int l31 = lane & 31, hf = lane >> 5;
  float* stat = (float*)(smem + QOFF + 8192);
  f32x16 sacc;
  float Mh = 0.f;
  if (wave < 4) {
#pragma unroll
    for (int i = 0; i < 16; ++i) sacc[i] = 0.f;
    u32x4 av[2][4], bv[2][4];
#define S_LOAD(BT)                                                                              \
  _Pragma("unroll") for (int u = 0; u < 4; ++u) {                                               \
    const u32 ks = 4 * (BT) + u, cb = ks >> 3, ss = ks & 7;                                      \
    av[(BT) & 1][u] = *(const u32x4*)(smem + cb * 32768 + off_b(32 * wave + l31, 2 * ss + hf));  \
    bv[(BT) & 1][u] = *(const u32x4*)(smem + QOFF + cb * 8192 + off_b(l31, 2 * ss + hf));        \
  }
    S_LOAD(0)
#pragma unroll
    for (int bt = 0; bt < 8; ++bt) {
      if (bt < 7) { S_LOAD(bt + 1) }
      SCHED();
#pragma unroll
      for (int u = 0; u < 4; ++u) sacc = mfma32(av[bt & 1][u], bv[bt & 1][u], sacc);
      SCHED();
    }
    float mx = -INFINITY;
#pragma unroll
    for (int i = 0; i < 16; ++i) { sacc[i] *= C2; mx = fmaxf(mx, sacc[i]); }
    Mh = x32_max(mx);
  }
  BAR();
  if (wave < 4 && lane < 32) stat[wave * 32 + lane] = Mh;
  BAR();
  if (wave < 4) {
    Mh = fmaxf(fmaxf(stat[l31], stat[32 + l31]), fmaxf(stat[64 + l31], stat[96 + l31]));
    float ls = 0.f;
#pragma unroll
    for (int i = 0; i < 16; ++i) { sacc[i] = ex2(sacc[i] - Mh); ls += sacc[i]; }
    ls = x32_sum(ls);
    if (lane < 32) stat[128 + wave * 32 + lane] = ls;
#pragma unroll
    for (int g = 0; g < 4; ++g) {
      const u32x2 pv = {pk2(sacc[4 * g], sacc[4 * g + 1]), pk2(sacc[4 * g + 2], sacc[4 * g + 3])};
      *(u32x2*)(smem + QOFF + off_b(l31, 4 * wave + g) + 8 * hf) = pv;
    }
  }
  BAR();
  const float Lh = (stat[128 + l31] + stat[160 + l31]) + (stat[192 + l31] + stat[224 + l31]);
  {
    f32x16 oacc;
#pragma unroll
    for (int i = 0; i < 16; ++i) oacc[i] = 0.f;
    const u32 cb = wave >> 2, ct = wave & 3;
    const u32 blk = (lane >> 4) & 1, q4 = (lane & 15) >> 2, p4 = lane & 3;
    u32x4 av[8], bv[8];
#pragma unroll
    for (int ks = 0; ks < 8; ++ks) {
      av[ks] = tr_pair(smem, cb * 32768 + off_b(16 * ks + 8 * hf + q4, 4 * ct + 2 * blk + (p4 >> 1)) + 8 * (p4 & 1),
                             cb * 32768 + off_b(16 * ks + 8 * hf + 4 + q4, 4 * ct + 2 * blk + (p4 >> 1)) + 8 * (p4 & 1));
      bv[ks] = *(const u32x4*)(smem + QOFF + off_b(l31, 2 * ks + hf));
    }
    SCHED();
#pragma unroll
    for (int ks = 0; ks < 8; ++ks) oacc = mfma32(av[ks], bv[ks], oacc);
    const float li = 1.f / Lh;
    BAR();
#pragma unroll
    for (int g = 0; g < 4; ++g) {
      const u32x2 ov = {pk2(oacc[4 * g] * li, oacc[4 * g + 1] * li), pk2(oacc[4 * g + 2] * li, oacc[4 * g + 3] * li)};
      *(u32x2*)(smem + 81920 + l31 * 1040 + 64 * wave + 16 * g + 8 * hf) = ov;
    }
  }
  BAR();
#pragma unroll
  for (int u = 0; u < 2; ++u) {
    const int h = 2 * wave + u;
    const u32x4 v = *(const u32x4*)(smem + 81920 + h * 1040 + lane * 16);
    st_sc1(rs, WS_OLAT + (u32)(((h * 2 + b) * 32 + s) * 1024 + lane * 16), v);
  }
  if (t < 32) st8_sc1(p.ws, WS_ML + (u32)(((t * 2 + b) * 32 + s) * 8), ((u64)__float_as_uint(Lh) << 32) | __float_as_uint(Mh));
  DRAIN();
  BAR();
  if (t < 64) st8_sc1(p.ws, WS_F2FLAG2 + t * 512 + j * 8, tag);
  if (wave >= 8) { wo_dma(p.woA + (size_t)(64 * j) * 576, lds0, wave - 8, lane); DRAIN(); }
  float* ag = (float*)(smem + QOFF);
  constexpr u32 ZIMG = QOFF + 4096;
  if (t < 264) {
    const f32x2 v = spin_pair(rs, WS_AG + (u32)(j * 8 * 66 * 8 + t * 16), tag32);
    *(f32x2*)(ag + 2 * t) = v;
  }
  BAR();
  if (t < 128) {
    const int bb = t >> 6, d = t & 63;
    const float* a = ag + bb * 4 * 66;
    float M = a[64];
#pragma unroll
    for (int u = 1; u < 4; ++u) M = fmaxf(M, a[u * 66 + 64]);
    float L = 0.f, z = 0.f;
#pragma unroll
    for (int u = 0; u < 4; ++u) { const float wu = ex2(a[u * 66 + 64] - M); L = fmaf(wu, a[u * 66 + 65], L); z = fmaf(wu, a[u * 66 + d], z); }
    z /= L;
    z_store(smem, ZIMG, bb, d, z);
  }
  BAR();
  oproj_mfma(smem, 0, ZIMG, rs, oslot, tag32, wave, lane);
}

DI void role_F1(const Params& p, unsigned char* smem, __amdgpu_buffer_rsrc_t rs, u64 epoch, int w) {
  const int t = threadIdx.x, lane = t & 63, wave = __builtin_amdgcn_readfirstlane(t >> 6);
  const u64 t_start = __builtin_amdgcn_s_memrealtime();
  const int h = w & 31, dh = w >> 5;
  const int col0 = h * 128 + dh * 64;
  constexpr u32 UV_OFF = 0, WO_OFF = 65536, AI_OFF = 139264, SM_OFF = 155648;
  constexpr u32 ZIMG = SM_OFF + 2048;
  float* red = (float*)(smem + AI_OFF);
  unsigned char* qsh = smem + AI_OFF + 5120;
  float* qas = (float*)(smem + AI_OFF + 8192);
  float* part = (float*)(smem + SM_OFF);
  float* gsh = (float*)(smem + SM_OFF + 2560);
  float* fin = (float*)(smem + SM_OFF + 3072);
  const u32 lds0 = (u32)(size_t)smem;
  const u32 oslot = WS_OG + (u32)((64 + w) * 9216);
  u32x4 uk[4];
  const int ch = t & 7;
  if (wave < 9) {
    Gemv1 g;
    gemv1_issue(g, p.wqB + col0, p.x, t);
    WAITV(4);
#pragma unroll
    for (int k = 0; k < 4; ++k) uk[k] = ldg16nt(p.wuk + (size_t)((t >> 3) + 128 * k) * 4096 + col0 + 8 * ch);
    gemv1_finish(g, red, wave, lane);
  } else {
    __builtin_amdgcn_s_sleep(40);
#pragma unroll
    for (int k = 0; k < 4; ++k) uk[k] = ldg16nt(p.wuk + (size_t)((t >> 3) + 128 * k) * 4096 + col0 + 8 * ch);
    if (wave >= 14) {
      const u16* xr = p.x + (wave - 14) * 576;
      float sg = 0.f;
#pragma unroll
      for (int k = 0; k < 9; ++k) {
        const int mm = lane + 64 * k;
        sg = fmaf(__uint_as_float((u32)xr[mm] << 16), __uint_as_float((u32)p.gw[mm] << 16), sg);
      }
      sg += dpp<XOR1>(sg); sg += dpp<XOR2>(sg); sg += dpp<ROR4>(sg); sg += dpp<ROR8>(sg);
      sg = x32_sum(x16_sum(sg));
      if (lane == 0) gsh[wave - 14] = 1.f / (1.f + __expf(-sg));
    }
  }
  const u64 tag = tag_of(epoch);
  const u32 tag32 = (u32)tag;
  BAR();
  if (t < 16) *(u32x4*)(qsh + t * 16) = gemv1_sum(red, t);
  BAR();
  {
    const u32x4 q0 = *(const u32x4*)(qsh + ch * 16), q1 = *(const u32x4*)(qsh + 128 + ch * 16);
    float x[8];
#pragma unroll
    for (int k = 0; k < 4; ++k) { x[2 * k] = dot8(uk[k], q0); x[2 * k + 1] = dot8(uk[k], q1); }
    const int c = ch;
    TR_STEP(4, XOR7, 4) TR_STEP(2, XOR2, 2) TR_STEP(1, XOR1, 1)
    qas[(ch & 1) * 512 + (t >> 3) + 128 * (ch >> 1)] = x[0];
  }
  BAR();
  if (wave < 2) {
    const f32x4 v0 = *(const f32x4*)(qas + t * 8), v1 = *(const f32x4*)(qas + t * 8 + 4);
    const u32x4 o = {pk2(v0[0], v0[1]), pk2(v0[2], v0[3]), pk2(v1[0], v1[1]), pk2(v1[2], v1[3])};
    st_sc1(rs, WS_QABS + (u32)((((t >> 6) * 2 + dh) * 32 + h) * 1024 + (t & 63) * 16), o);
    DRAIN();
  }
  BAR();
  if (t < 64) st8_sc1(p.ws, WS_F1FLAG + t * 512 + w * 8, tag);
  float* rst = (float*)(smem + SM_OFF + 2624);
  if (t < 576) st_sc1(rs, oslot + t * 16, u32x4{0u, ~tag32, 0u, ~tag32});
  DRAIN();
  BAR();
  if (t == 0) st8_sc1(p.ws, WS_F1FLAGC + w * 8, tag);
  while (__builtin_amdgcn_s_memrealtime() - t_start < LATE_DMA) __builtin_amdgcn_s_sleep(4);
  if (wave < 8) {
    wo_dma(p.woB + (size_t)col0 * 576, lds0 + WO_OFF, wave, lane);
  } else {
#pragma unroll
    for (int it = 0; it < 8; ++it) {
      const int n = (wave - 8) * 8 + it;
      const u32 row = n * 8 + (lane >> 3);
      glds16(p.wuv + (size_t)row * 4096 + col0 + (((lane & 7) ^ wsw(row)) << 3), lds0 + UV_OFF + n * 1024);
    }
  }
  {
    const int bb = wave >> 3, sg = wave & 7;
    if (wave == 0) poll_flags(p.ws, WS_F1FLAGC + lane * 8, true, tag);
    poll_flags(p.ws, WS_F2FLAG2 + w * 512 + (2 * (4 * sg + (lane & 3)) + bb) * 8, lane < 4, tag);
    u32x4 pv[4];
#pragma unroll
    for (int u = 0; u < 4; ++u) pv[u] = ld_sc1(rs, WS_OLAT + (u32)(((h * 2 + bb) * 32 + 4 * sg + u) * 1024 + lane * 16));
    const u64 ml = ld8_sc1(p.ws, WS_ML + (u32)(((h * 2 + bb) * 32 + 4 * sg + (lane & 3)) * 8));
    const float ms = __uint_as_float((u32)ml), ls = __uint_as_float((u32)(ml >> 32));
    float M = ms;
    M = fmaxf(M, dpp<XOR1>(M)); M = fmaxf(M, dpp<XOR2>(M));
    const float wl = ls * ex2(ms - M);
    float L = wl;
    L += dpp<XOR1>(L); L += dpp<XOR2>(L);
    if (lane == 0) *(f32x2*)(rst + wave * 2) = f32x2{M, L};
    float a[8];
#pragma unroll
    for (int e = 0; e < 8; ++e) a[e] = 0.f;
#pragma unroll
    for (int u = 0; u < 4; ++u) {
      const float wu = u == 0 ? dpp<0x00>(wl) : u == 1 ? dpp<0x55>(wl) : u == 2 ? dpp<0xAA>(wl) : dpp<0xFF>(wl);
#pragma unroll
      for (int e = 0; e < 8; ++e) a[e] = fmaf(wu, (e & 1) ? bfhi(pv[u][e >> 1]) : bflo(pv[u][e >> 1]), a[e]);
    }
    *(u32x4*)(smem + AI_OFF + (lane >> 4) * 4096 + off_b(wave, lane & 15)) =
        u32x4{pk2(a[0], a[1]), pk2(a[2], a[3]), pk2(a[4], a[5]), pk2(a[6], a[7])};
    DRAIN();
  }
  BAR();
  {
    const int nt = wave & 3, kq = wave >> 2;
    u32x4 af[4], bf[4];
#pragma unroll
    for (int u = 0; u < 4; ++u) {
      af[u] = *(const u32x4*)(smem + AI_OFF + kq * 4096 + off_b(lane & 15, 4 * u + (lane >> 4)));
      bf[u] = wfrag(smem, UV_OFF, 128, 4 * kq + u, nt, lane);
    }
    const float* rb = rst + (lane >> 5) * 16;
    float M = rb[0];
#pragma unroll
    for (int u = 1; u < 8; ++u) M = fmaxf(M, rb[2 * u]);
    float L = 0.f;
#pragma unroll
    for (int u = 0; u < 8; ++u) L = fmaf(ex2(rb[2 * u] - M), rb[2 * u + 1], L);
    const float* rr = rst + (lane >> 4) * 8;
    const float li = 1.f / L;
    f32x4 acc = {0.f, 0.f, 0.f, 0.f};
#pragma unroll
    for (int u = 0; u < 4; ++u) acc = mfma16(af[u], bf[u], acc);
    float sv = 0.f;
#pragma unroll
    for (int u = 0; u < 4; ++u) sv = fmaf(acc[u], ex2(rr[2 * u] - M), sv);
    sv = x16_sum(sv) * li;
    if ((lane & 16) == 0) part[(kq * 2 + (lane >> 5)) * 64 + nt * 16 + (lane & 15)] = sv;
  }
  BAR();
  if (t < 128) z_store(smem, ZIMG, t >> 6, t & 63, (part[t] + part[128 + t]) + (part[256 + t] + part[384 + t]));
  BAR();
  oproj_mfma(smem, WO_OFF, ZIMG, rs, oslot, tag32, wave, lane);
  if (t < 576) {
    const int pp = t / 9, i = t - pp * 9;
    const u32 off = WS_OG + (u32)(pp * 9216 + (9 * w + i) * 16);
    const f32x2 vb = spin_pair(rs, off + 64 * 9216, tag32);
    const f32x2 va = spin_pair(rs, off, tag32);
    const float g0 = gsh[0], g1 = gsh[1];
    *(f32x2*)(fin + 2 * t) = f32x2{g0 * va[0] + (1.f - g0) * vb[0], g1 * va[1] + (1.f - g1) * vb[1]};
  }
  BAR();
  if (t < 192) {
    const int o = t >> 3, k = t & 7;
    float sum = 0.f;
    if (o < 18) {
#pragma unroll
      for (int u = 0; u < 8; ++u) sum += fin[(k * 8 + u) * 18 + o];
    }
    sum += dpp<XOR1>(sum); sum += dpp<XOR2>(sum); sum += dpp<XOR7>(sum);
    if (k == 0 && o < 18) p.out[(o & 1) * 576 + 9 * w + (o >> 1)] = bf1(sum);
    if (w == 0 && t == 0) st8_sc1(p.ws, WS_EPOCH, tag);
  }
}

__global__ void __launch_bounds__(1024, 4) fused_kernel(Params p) {
  __shared__ __attribute__((aligned(16))) unsigned char smem[LDS_BYTES];
  const u64 epoch = __hip_atomic_load((GAS u64*)(p.ws + WS_EPOCH), __ATOMIC_RELAXED, __HIP_MEMORY_SCOPE_WORKGROUP);
  const __amdgpu_buffer_rsrc_t rs = __builtin_amdgcn_make_buffer_rsrc(p.ws, 0, WS_END, 0x00020000);
  const int w = blockIdx.x;
  if (w < 64) role_F1(p, smem, rs, epoch, w);
  else if (w < 128) role_F2(p, smem, rs, epoch, w - 64);
  else role_A(p, smem, rs, epoch, w - 128);
}


}

extern "C" void kernel_launch(void* const* d_in, const int* in_sizes, int n_in,
                              void* d_out, int out_size, void* d_ws, size_t ws_size,
                              hipStream_t stream) {
  Params p;
  p.x = (const u16*)d_in[0];
  p.K = (const u16*)d_in[1];
  p.V = (const u16*)d_in[2];
  p.wqA = (const u16*)d_in[3];
  p.woA = (const u16*)d_in[4];
  p.ckv = (const u16*)d_in[6];
  p.wqB = (const u16*)d_in[7];
  p.wuk = (const u16*)d_in[8];
  p.wuv = (const u16*)d_in[9];
  p.woB = (const u16*)d_in[10];
  p.gw = (const u16*)d_in[11];
  p.out = (u16*)d_out;
  p.ws = (unsigned char*)d_ws;
  hipLaunchKernelGGL(fused_kernel, dim3(256), dim3(1024), 0, stream, p);
}
```
